```python
import torch

def _softmax(x, axis=-1):
    return torch.softmax(x, dim=axis)
def _gelu(x):
    return torch.nn.functional.gelu(x, approximate="tanh")
def _silu(x):
    return torch.nn.functional.silu(x)
def _sigmoid(x):
    return torch.sigmoid(x)
def _layernorm(x, axis=-1, eps=1e-5):
    m = x.mean(dim=axis, keepdim=True)
    v = x.var(dim=axis, keepdim=True, unbiased=False)
    return (x - m) / torch.sqrt(v + eps)
def _scatter_add(tgt, idx, src, axis=0):
    return tgt.clone().index_add_(axis, idx, src)
def _segment_sum(src, seg, n):
    out = torch.zeros((n,) + src.shape[1:], dtype=src.dtype)
    return out.index_add_(0, seg, src)
def _pad(x, pw):
    flat = tuple(v for pair in reversed(pw) for v in pair)
    return torch.nn.functional.pad(x, flat)

kernel_name = 'gated_mix__gqa_block_cached__gqa_block_cached__decode'

def setup_inputs():

    _w = (lambda a: (lambda t: t.to(torch.bfloat16) if t.is_floating_point() else t)(torch.as_tensor(a)))
    def _A_setup():
        B, Sq, D = 4, 1, 1024
        Hq, Hkv, Sk, Dh = 32, 4, 8192, 128
        assert Hq % Hkv == 0, (Hq, Hkv)
        return {
            "x": _w((lambda *s: torch.randn(*s, dtype=torch.bfloat16) * 0.05)(B, Sq, D)),
            "k_cache": _w((lambda *s: torch.randn(*s, dtype=torch.bfloat16) * 6.4)(B, Hkv, Sk, Dh)),
            "v_cache": _w((lambda *s: torch.randn(*s, dtype=torch.bfloat16) * 0.05)(B, Hkv, Sk, Dh)),
            "wq": _w((lambda *s: torch.randn(*s, dtype=torch.bfloat16) * 6.4)(D, Hq * Dh) * (D ** -0.5)),
            "wo": _w((lambda *s: torch.randn(*s, dtype=torch.bfloat16) * 0.05)(Hq * Dh, D) * ((Hq * Dh) ** -0.5)),
        }
    _A_in = _A_setup()

    def _B_setup():
        B, Sq, D = 4, 1, 1024
        Hq, Hkv, Sk, Dh = 32, 4, 8192, 128
        assert Hq % Hkv == 0, (Hq, Hkv)
        return {
            "x": _w((lambda *s: torch.randn(*s, dtype=torch.bfloat16) * 0.05)(B, Sq, D)),
            "k_cache": _w((lambda *s: torch.randn(*s, dtype=torch.bfloat16) * 6.4)(B, Hkv, Sk, Dh)),
            "v_cache": _w((lambda *s: torch.randn(*s, dtype=torch.bfloat16) * 0.05)(B, Hkv, Sk, Dh)),
            "wq": _w((lambda *s: torch.randn(*s, dtype=torch.bfloat16) * 6.4)(D, Hq * Dh) * (D ** -0.5)),
            "wo": _w((lambda *s: torch.randn(*s, dtype=torch.bfloat16) * 0.05)(Hq * Dh, D) * ((Hq * Dh) ** -0.5)),
        }
    _B_in = _B_setup()

    return {
        "A_x": _A_in["x"],
        "A_k_cache": _A_in["k_cache"],
        "A_v_cache": _A_in["v_cache"],
        "A_wq": _A_in["wq"],
        "A_wo": _A_in["wo"],
        "B_x": _B_in["x"],
        "B_k_cache": _B_in["k_cache"],
        "B_v_cache": _B_in["v_cache"],
        "B_wq": _B_in["wq"],
        "B_wo": _B_in["wo"],
        "gate_w": _w((lambda *s: torch.randn(*s, dtype=torch.bfloat16) * 0.05)(1024) * 16.0),
    }

def reference(A_x, A_k_cache, A_v_cache, A_wq, A_wo, B_x, B_k_cache, B_v_cache, B_wq, B_wo, gate_w):
    def _A_ref(x, k_cache, v_cache, wq, wo):
        B, Sq, D = x.shape
        Hq, Dh = 32, 128
        Hkv = k_cache.shape[1]
        rep = Hq // Hkv
        q = (lambda x, *d: x.permute(*d))((x @ wq).reshape(B, Sq, Hq, Dh), 0, 2, 1, 3)
        k = (lambda x, n, axis: torch.repeat_interleave(x, n, dim=axis))(k_cache, rep, axis=1)
        v = (lambda x, n, axis: torch.repeat_interleave(x, n, dim=axis))(v_cache, rep, axis=1)
        scores = torch.einsum("bhqd,bhkd->bhqk", q, k) * (Dh ** -0.5)
        p = _softmax(scores, axis=-1)
        o = torch.einsum("bhqk,bhkd->bhqd", p, v)
        return (lambda x, *d: x.permute(*d))(o, 0, 2, 1, 3).reshape(B, Sq, Hq * Dh) @ wo

    def _B_ref(x, k_cache, v_cache, wq, wo):
        B, Sq, D = x.shape
        Hq, Dh = 32, 128
        Hkv = k_cache.shape[1]
        rep = Hq // Hkv
        q = (lambda x, *d: x.permute(*d))((x @ wq).reshape(B, Sq, Hq, Dh), 0, 2, 1, 3)
        k = (lambda x, n, axis: torch.repeat_interleave(x, n, dim=axis))(k_cache, rep, axis=1)
        v = (lambda x, n, axis: torch.repeat_interleave(x, n, dim=axis))(v_cache, rep, axis=1)
        scores = torch.einsum("bhqd,bhkd->bhqk", q, k) * (Dh ** -0.5)
        p = _softmax(scores, axis=-1)
        o = torch.einsum("bhqk,bhkd->bhqd", p, v)
        return (lambda x, *d: x.permute(*d))(o, 0, 2, 1, 3).reshape(B, Sq, Hq * Dh) @ wo

    x = A_x
    g = _sigmoid((x * gate_w).sum(-1, keepdims=True))
    a = _A_ref(x, A_k_cache, A_v_cache, A_wq, A_wo)
    b = _B_ref(x, B_k_cache, B_v_cache, B_wq, B_wo)
    return g * a + (1 - g) * b
```

```cpp
#include <hip/hip_runtime.h>
#include <hip/hip_bf16.h>
#include <stdint.h>

typedef unsigned short u16;
typedef unsigned int u32;
typedef u32 u32x4 __attribute__((ext_vector_type(4)));
typedef float f32x4 __attribute__((ext_vector_type(4)));

constexpr int NB = 4, DM = 1024, HQ = 32, HKV = 4, SK = 8192, DH = 128, NQ = HQ * DH;
constexpr int NSPLIT = 8, KEYS = SK / NSPLIT;

__device__ __forceinline__ float bf2f(u16 v) { return __uint_as_float((u32)v << 16); }
__device__ __forceinline__ u16 f2bf(float f) { u32 u = __float_as_uint(f); u += 0x7FFFu + ((u >> 16) & 1u); return (u16)(u >> 16); }
__device__ __forceinline__ float rbf(float f) { return bf2f(f2bf(f)); }

constexpr size_t WS_Q = 0;
constexpr size_t WS_PM = WS_Q + 2 * NB * NQ;
constexpr size_t WS_PL = WS_PM + 2 * NB * HQ * NSPLIT;
constexpr size_t WS_PO = WS_PL + 2 * NB * HQ * NSPLIT;
constexpr size_t WS_OUT = WS_PO + (size_t)2 * NB * HQ * NSPLIT * DH;

__global__ void __launch_bounds__(256) k_q(const u16* x, const u16* wqA, const u16* wqB, float* ws) {
  const int c = blockIdx.x, br = c >> 7, col0 = (c & 127) * 32;
  const u16* wq = br ? wqB : wqA;
  const int tid = threadIdx.x, col = tid & 31, kq = tid >> 5;
  float acc[4] = {0, 0, 0, 0};
  for (int k = kq * 128; k < kq * 128 + 128; ++k) {
    const float w = bf2f(wq[(size_t)k * NQ + col0 + col]);
    for (int b = 0; b < 4; ++b) acc[b] += bf2f(x[b * DM + k]) * w;
  }
  __shared__ float red[8][4][32];
  for (int b = 0; b < 4; ++b) red[kq][b][col] = acc[b];
  __syncthreads();
  if (tid < 128) {
    const int b = tid >> 5;
    float s = 0;
    for (int i = 0; i < 8; ++i) s += red[i][b][col];
    ws[WS_Q + ((size_t)br * NB + b) * NQ + col0 + col] = rbf(s);
  }
}

__global__ void __launch_bounds__(256) k_attn(const u16* kA, const u16* vA, const u16* kB, const u16* vB, float* ws) {
  const int c = blockIdx.x, s = c & 7, kvh = (c >> 3) & 3, b = (c >> 5) & 3, br = c >> 7;
  const u16* K = (br ? kB : kA) + ((size_t)(b * HKV + kvh) * SK + s * KEYS) * DH;
  const u16* V = (br ? vB : vA) + ((size_t)(b * HKV + kvh) * SK + s * KEYS) * DH;
  __shared__ float q[8][DH];
  __shared__ float sc[8][KEYS];
  __shared__ float mx[8], sm[8];
  const int tid = threadIdx.x;
  for (int i = tid; i < 8 * DH; i += 256) q[i / DH][i % DH] = ws[WS_Q + ((size_t)br * NB + b) * NQ + kvh * 1024 + i];
  __syncthreads();
  for (int key = tid; key < KEYS; key += 256) {
    float a[8] = {0, 0, 0, 0, 0, 0, 0, 0};
    for (int d = 0; d < DH; d += 8) {
      const u32x4 kv = *(const u32x4*)(K + (size_t)key * DH + d);
      for (int e = 0; e < 4; ++e) {
        const float lo = __uint_as_float(kv[e] << 16), hi = __uint_as_float(kv[e] & 0xffff0000u);
        for (int h = 0; h < 8; ++h) a[h] += lo * q[h][d + 2 * e] + hi * q[h][d + 2 * e + 1];
      }
    }
    for (int h = 0; h < 8; ++h) sc[h][key] = a[h] * 0.08838834764831845f;
  }
  __syncthreads();
  {
    const int h = tid >> 5, l = tid & 31;
    float m = -INFINITY;
    for (int k = l; k < KEYS; k += 32) m = fmaxf(m, sc[h][k]);
    for (int o = 16; o; o >>= 1) m = fmaxf(m, __shfl_xor(m, o));
    float t = 0;
    for (int k = l; k < KEYS; k += 32) { const float p = __expf(sc[h][k] - m); sc[h][k] = p; t += p; }
    for (int o = 16; o; o >>= 1) t += __shfl_xor(t, o);
    if (l == 0) { mx[h] = m; sm[h] = t; }
  }
  __syncthreads();
  {
    const int d = tid & 127, hh = tid >> 7;
    float a[4] = {0, 0, 0, 0};
    for (int k = 0; k < KEYS; ++k) {
      const float v = bf2f(V[(size_t)k * DH + d]);
      for (int j = 0; j < 4; ++j) a[j] += sc[hh * 4 + j][k] * v;
    }
    for (int j = 0; j < 4; ++j) {
      const size_t row = (((size_t)br * NB + b) * HQ + kvh * 8 + hh * 4 + j) * NSPLIT + s;
      ws[WS_PO + row * DH + d] = a[j];
      if (d == 0) { ws[WS_PM + row] = mx[hh * 4 + j]; ws[WS_PL + row] = sm[hh * 4 + j]; }
    }
  }
}

__global__ void __launch_bounds__(256) k_wo(const u16* woA, const u16* woB, float* ws) {
  const int c = blockIdx.x, ns = c & 3, h = (c >> 2) & 31, br = c >> 7;
  const u16* wo = br ? woB : woA;
  __shared__ float o[4][DH];
  const int tid = threadIdx.x;
  for (int i = tid; i < 4 * DH; i += 256) {
    const int b = i >> 7, d = i & 127;
    const size_t row = (((size_t)br * NB + b) * HQ + h) * NSPLIT;
    float M = -INFINITY;
    for (int s = 0; s < NSPLIT; ++s) M = fmaxf(M, ws[WS_PM + row + s]);
    float L = 0, a = 0;
    for (int s = 0; s < NSPLIT; ++s) { const float w = __expf(ws[WS_PM + row + s] - M); L += w * ws[WS_PL + row + s]; a += w * ws[WS_PO + (row + s) * DH + d]; }
    o[b][d] = rbf(a / L);
  }
  __syncthreads();
  const int n = ns * 256 + tid;
  float acc[4] = {0, 0, 0, 0};
  for (int d = 0; d < DH; ++d) {
    const float w = bf2f(wo[(size_t)(h * DH + d) * DM + n]);
    for (int b = 0; b < 4; ++b) acc[b] += o[b][d] * w;
  }
  for (int b = 0; b < 4; ++b) ws[WS_OUT + (((size_t)br * HQ + h) * NB + b) * DM + n] = acc[b];
}

__global__ void __launch_bounds__(256) k_fin(const u16* x, const u16* gw, const float* ws, u16* out) {
  const int b = blockIdx.x >> 2, n = (blockIdx.x & 3) * 256 + threadIdx.x;
  __shared__ float red[256];
  float t = 0;
  for (int k = threadIdx.x; k < DM; k += 256) t += rbf(bf2f(x[b * DM + k]) * bf2f(gw[k]));
  red[threadIdx.x] = t;
  __syncthreads();
  for (int o = 128; o; o >>= 1) { if (threadIdx.x < o) red[threadIdx.x] += red[threadIdx.x + o]; __syncthreads(); }
  const float z = rbf(red[0]);
  const float g = rbf(1.f / (1.f + __expf(-z)));
  float a = 0, bb = 0;
  for (int h = 0; h < HQ; ++h) { a += ws[WS_OUT + ((size_t)h * NB + b) * DM + n]; bb += ws[WS_OUT + (((size_t)HQ + h) * NB + b) * DM + n]; }
  a = rbf(a); bb = rbf(bb);
  out[b * DM + n] = f2bf(rbf(g * a) + rbf(rbf(1.f - g) * bb));
}

extern "C" void kernel_launch(void* const* d_in, const int* in_sizes, int n_in, void* d_out, int out_size, void* d_ws, size_t ws_size,
                              hipStream_t stream) {
  const u16* x = (const u16*)d_in[0];
  float* ws = (float*)d_ws;
  k_q<<<256, 256, 0, stream>>>(x, (const u16*)d_in[3], (const u16*)d_in[8], ws);
  k_attn<<<256, 256, 0, stream>>>((const u16*)d_in[1], (const u16*)d_in[2], (const u16*)d_in[6], (const u16*)d_in[7], ws);
  k_wo<<<256, 256, 0, stream>>>((const u16*)d_in[4], (const u16*)d_in[9], ws);
  k_fin<<<16, 256, 0, stream>>>(x, (const u16*)d_in[10], ws, (u16*)d_out);
}
```

```cpp
#include <hip/hip_runtime.h>
#include <hip/hip_bf16.h>
#include <stdint.h>

typedef unsigned short u16;
typedef unsigned int u32;
typedef unsigned long long u64;
typedef u32 u32x2 __attribute__((ext_vector_type(2)));
typedef u32 u32x4 __attribute__((ext_vector_type(4)));
typedef float f32x2 __attribute__((ext_vector_type(2)));
typedef float f32x4 __attribute__((ext_vector_type(4)));
typedef short bf16x8 __attribute__((ext_vector_type(8)));
typedef __amdgpu_buffer_rsrc_t rsrc_t;
#define LAS __attribute__((address_space(3)))

#define P_D1 6
#define P_W 24
#define P_POLL 16
#define P_PACE 6

constexpr int KV_BYTES = 4 * 4 * 8192 * 128 * 2;
constexpr int W_BYTES = 1024 * 4096 * 2;
constexpr int AUX_NT = 2, AUX_SC1 = 16;

constexpr int WS_EPOCH = 0;
constexpr int WS_Q = 4096;
constexpr int WS_OD = WS_Q + 2 * 4 * 4 * 512 * 8;
constexpr int WS_OS = WS_OD + 2 * 32 * 4 * 8 * 64 * 8;
constexpr int WS_F = WS_OS + 2 * 32 * 4 * 8 * 16;
constexpr int WS_END = WS_F + 256 * 64 * 16 * 8;
constexpr int WS_STAMP = 8 << 20;

constexpr int L_KST = 0;
constexpr int L_RED = L_KST + 8 * 8192;
constexpr int L_QS = L_RED + 16384;
constexpr int PSTR = 132;
constexpr int L_PART = L_QS + 2048;
constexpr int L_ML = L_PART + 8 * 8 * PSTR * 4;
constexpr int L_OB = L_ML + 512;
constexpr int L_GP = L_OB + 1024;
constexpr int L_END = L_GP + 128;

struct Params {
  const void* x; const void* gw;
  const void* k0; const void* v0; const void* wq0; const void* wo0;
  const void* k1; const void* v1; const void* wq1; const void* wo1;
  void* out; void* ws;
};

__device__ __forceinline__ rsrc_t mkr(const void* p, int bytes) { return __builtin_amdgcn_make_buffer_rsrc((void*)p, 0, bytes, 0x00020000); }
__device__ __forceinline__ u32x4 ldnt(rsrc_t r, int voff, int soff) { return __builtin_amdgcn_raw_buffer_load_b128(r, voff, soff, AUX_NT); }
__device__ __forceinline__ u32x4 ldpl(rsrc_t r, int voff, int soff) { return __builtin_amdgcn_raw_buffer_load_b128(r, voff, soff, 0); }
__device__ __forceinline__ u32x4 ldc4(rsrc_t r, int voff, int soff) { return __builtin_amdgcn_raw_buffer_load_b128(r, voff, soff, AUX_SC1); }
__device__ __forceinline__ u32x2 ldc2(rsrc_t r, int voff, int soff) { return __builtin_amdgcn_raw_buffer_load_b64(r, voff, soff, AUX_SC1); }
__device__ __forceinline__ void stc4(u32x4 v, rsrc_t r, int voff, int soff) { __builtin_amdgcn_raw_buffer_store_b128(v, r, voff, soff, AUX_SC1); }
__device__ __forceinline__ void stc2(u32x2 v, rsrc_t r, int voff, int soff) { __builtin_amdgcn_raw_buffer_store_b64(v, r, voff, soff, AUX_SC1); }
__device__ __forceinline__ f32x4 mfma(u32x4 a, u32x4 b, f32x4 c) {
  return __builtin_amdgcn_mfma_f32_16x16x32_bf16(__builtin_bit_cast(bf16x8, a), __builtin_bit_cast(bf16x8, b), c, 0, 0, 0);
}
__device__ __forceinline__ float asf(u32 u) { return __builtin_bit_cast(float, u); }
__device__ __forceinline__ u32 asu(float f) { return __builtin_bit_cast(u32, f); }
typedef __bf16 bf2_t __attribute__((ext_vector_type(2)));
__device__ __forceinline__ u32 pkbf(float lo, float hi) {
  const f32x2 v = {lo, hi};
  return __builtin_bit_cast(u32, __builtin_convertvector(v, bf2_t));
}
__device__ __forceinline__ float rbf(float f) { return asf(pkbf(f, 0.f) << 16); }
__device__ __forceinline__ float bflo(u32 u) { return asf(u << 16); }
__device__ __forceinline__ float bfhi(u32 u) { return asf(u & 0xffff0000u); }
__device__ __forceinline__ float x16(float v, bool mx) {
  auto s = __builtin_amdgcn_permlane16_swap(asu(v), asu(v), false, false);
  return mx ? fmaxf(asf(s[0]), asf(s[1])) : asf(s[0]) + asf(s[1]);
}
__device__ __forceinline__ float x32(float v, bool mx) {
  auto s = __builtin_amdgcn_permlane32_swap(asu(v), asu(v), false, false);
  return mx ? fmaxf(asf(s[0]), asf(s[1])) : asf(s[0]) + asf(s[1]);
}
template <int CTRL> __device__ __forceinline__ float dpp(float v) {
  return asf(__builtin_amdgcn_mov_dpp(asu(v), CTRL, 0xf, 0xf, true));
}
constexpr int XOR1 = 0xB1, XOR2 = 0x4E, XOR7 = 0x141, XOR8 = 0x128;
#define SB() __builtin_amdgcn_sched_barrier(0)
#define PIN() do { asm volatile("" ::: "memory"); __builtin_amdgcn_sched_barrier(0); } while (0)
template <int N> __device__ __forceinline__ void waitvm() { asm volatile("s_waitcnt vmcnt(%0)" ::"n"(N) : "memory"); }
constexpr unsigned kSpin = 1u << 18;

__device__ __forceinline__ u32x4 colfrag(const u32x4* v, int j) {
  const u32 sel = (j & 1) ? 0x07060302u : 0x05040100u;
  const int d = j >> 1;
  u32x4 f;
  f[0] = __builtin_amdgcn_perm(v[1][d], v[0][d], sel);
  f[1] = __builtin_amdgcn_perm(v[3][d], v[2][d], sel);
  f[2] = __builtin_amdgcn_perm(v[5][d], v[4][d], sel);
  f[3] = __builtin_amdgcn_perm(v[7][d], v[6][d], sel);
  return f;
}

#define STAMP(i) do { } while (0)

__global__ void __launch_bounds__(512, 2) gated_mix_decode_kernel(Params p) {
  __shared__ __attribute__((aligned(16))) unsigned char smem[L_END];
  LAS unsigned char* L = (LAS unsigned char*)smem;
  const int tid = threadIdx.x, lane = tid & 63, w = __builtin_amdgcn_readfirstlane(tid >> 6);
  const int c = blockIdx.x, g = c & 31, s = c >> 5, br = g >> 4, b = (g >> 2) & 3, kvh = g & 3, m = b * 8 + s;
  const int g4 = lane >> 4, l16 = lane & 15;
  const rsrc_t rK = mkr(br ? p.k1 : p.k0, KV_BYTES), rV = mkr(br ? p.v1 : p.v0, KV_BYTES);
  const rsrc_t rWQ = mkr(br ? p.wq1 : p.wq0, W_BYTES), rWO = mkr(br ? p.wo1 : p.wo0, W_BYTES);
  const rsrc_t rX = mkr(p.x, 4 * 1024 * 2), rG = mkr(p.gw, 1024 * 2), rWS = mkr(p.ws, WS_END);
  STAMP(0);

  const int kvS = (b * 4 + kvh) * (8192 * 256) + (s * 256 + w * 4) * 1024;
  const int kvV = lane * 16;
  const int wqV = (32 * ((lane >> 2) & 3) + 8 * g4) * 8192 + (lane & 3) * 16;
  const int wqS = (128 * w) * 8192 + (kvh * 1024 + 32 * m) * 2;
  const int hO = kvh * 8 + (m >> 2), ns = m & 3;
  const int woV = (8 * g4) * 2048 + l16 * 16;
  const int woS = (hO * 128 + 32 * (w >> 1)) * 2048 + (256 * ns + 128 * (w & 1)) * 2;
  u32x4 st[80];
  auto issue = [&](int n) {
    if (n < 8) st[n] = ldnt(rWQ, wqV, wqS + n * 8192);
    else if (n < 40) { const int j = n - 8; st[n] = ldnt(rK, kvV, kvS + ((j >> 2) * 32 + (j & 3)) * 1024); }
    else if (n < 72) { const int j = n - 40; st[n] = ldnt(rV, kvV, kvS + ((j >> 2) * 32 + (j & 3)) * 1024); }
    else if (n < 80) st[n] = ldnt(rWO, woV, woS + (n - 72) * 2048);
  };

  const u32 epoch = __builtin_amdgcn_raw_buffer_load_b32(rWS, 0, WS_EPOCH, AUX_SC1);
  const int xk = 128 * w + 32 * (l16 >> 2) + 8 * g4;
  const u32x4 xf = ldpl(rX, ((lane & 3) * 1024 + xk) * 2, 0);
  const u32x4 gf = ldpl(rG, xk * 2, 0);
  PIN();
#pragma unroll
  for (int n = 0; n < 8 + P_D1; ++n) { issue(n); PIN(); }

  {
    f32x4 qa[8];
#pragma unroll
    for (int j = 0; j < 8; ++j) qa[j] = mfma(xf, colfrag(st, j), f32x4{0.f, 0.f, 0.f, 0.f});
    STAMP(1);
    if (g4 == ((lane >> 2) & 3)) {
      LAS float* red = (LAS float*)(L + L_RED) + ((w * 4 + g4) * 4) * 32 + (lane & 3) * 8;
#pragma unroll
      for (int r = 0; r < 4; ++r) {
        *(LAS f32x4*)(red + r * 32) = f32x4{qa[0][r], qa[1][r], qa[2][r], qa[3][r]};
        *(LAS f32x4*)(red + r * 32 + 4) = f32x4{qa[4][r], qa[5][r], qa[6][r], qa[7][r]};
      }
    }
    float gs = 0.f;
#pragma unroll
    for (int d = 0; d < 4; ++d) gs += rbf(bflo(xf[d]) * bflo(gf[d])) + rbf(bfhi(xf[d]) * bfhi(gf[d]));
    gs += dpp<0x124>(gs);
    gs += dpp<XOR8>(gs);
    gs = x16(gs, false);
    gs = x32(gs, false);
    if (lane < 4) ((LAS float*)(L + L_GP))[w * 4 + lane] = gs;
  }
  const u32 tag = epoch + 1u;
  __syncthreads();
  if (w == 0) {
    const LAS f32x2* red = (const LAS f32x2*)(L + L_RED) + g4 * 16 + l16;
    f32x2 a = {0.f, 0.f};
#pragma unroll
    for (int i = 0; i < 32; ++i) a += red[i * 64];
    stc2(u32x2{pkbf(a[0], a[1]), tag}, rWS, (g4 * 512 + l16) * 8, WS_Q + (((br * 4 + kvh) * 4) * 512 + m * 16) * 8);
    STAMP(2);
  }
  PIN();

  const int qS = WS_Q + (((br * 4 + kvh) * 4 + b) * 512 + w * 64) * 8;
#pragma unroll
  for (int n = 8 + P_D1; n < 8 + P_POLL; ++n) { if (P_PACE) waitvm<(P_PACE ? P_PACE - 1 : 0)>(); issue(n); PIN(); }
  u32x2 qg = ldc2(rWS, lane * 8, qS);
  PIN();
#pragma unroll
  for (int n = 8 + P_POLL; n < 8 + P_W; ++n) { issue(n); PIN(); }
  if (!__all(qg[1] == tag)) {
    for (unsigned spin = 0; spin < kSpin; ++spin) {
      __builtin_amdgcn_s_sleep(2);
      qg = ldc2(rWS, lane * 8, qS);
      if (__all(qg[1] == tag)) break;
    }
  }
  STAMP(3);
  ((LAS u32*)(L + L_QS))[tid] = qg[0];
  __syncthreads();
  u32x4 qf[4];
#pragma unroll
  for (int kk = 0; kk < 4; ++kk) qf[kk] = *(const LAS u32x4*)(L + L_QS + (l16 & 7) * 256 + kk * 64 + g4 * 16);
  STAMP(4);

  f32x4 S[8];
  {
    LAS unsigned char* kst = L + L_KST + w * 8192;
#pragma unroll
    for (int T = 0; T < 8; ++T) {
      LAS unsigned char* kb = kst + (T & 1) * 4096;
#pragma unroll
      for (int i = 0; i < 4; ++i) {
        const int rho = 4 * g4 + i;
        *(LAS u32x4*)(kb + rho * 256 + ((l16 ^ rho) * 16)) = st[8 + 4 * T + i];
      }
      PIN();
#pragma unroll
      for (int i = 0; i < 4; ++i) issue(8 + P_W + 4 * T + i);
      PIN();
      __builtin_amdgcn_fence(__ATOMIC_RELEASE, "wavefront");
      __builtin_amdgcn_wave_barrier();
      __builtin_amdgcn_fence(__ATOMIC_ACQUIRE, "wavefront");
      f32x4 a = {0.f, 0.f, 0.f, 0.f};
#pragma unroll
      for (int kk = 0; kk < 4; ++kk) {
        const u32x4 kf = *(const LAS u32x4*)(kb + l16 * 256 + (((4 * kk + g4) ^ l16) * 16));
        a = mfma(kf, qf[kk], a);
      }
      S[T] = a;
    }
  }
  STAMP(5);
  constexpr float SC = 0.08838834764831845f * 1.4426950408889634f;
  float mx = S[0][0];
#pragma unroll
  for (int T = 0; T < 8; ++T)
#pragma unroll
    for (int r = 0; r < 4; ++r) mx = fmaxf(mx, S[T][r]);
  mx = x32(x16(mx, true), true);
  const float mxs = mx * SC;
  float ls = 0.f;
  u32x4 pf[4];
#pragma unroll
  for (int T = 0; T < 8; ++T) {
    float e[4];
#pragma unroll
    for (int r = 0; r < 4; ++r) { e[r] = __builtin_amdgcn_exp2f(S[T][r] * SC - mxs); ls += e[r]; }
    pf[T >> 1][(T & 1) * 2] = pkbf(e[0], e[1]);
    pf[T >> 1][(T & 1) * 2 + 1] = pkbf(e[2], e[3]);
  }
  ls = x32(x16(ls, false), false);
  if (lane < 8) *(LAS f32x2*)(L + L_ML + (w * 8 + lane) * 8) = f32x2{mxs, ls};
  STAMP(6);
  f32x4 acc[8];
#pragma unroll
  for (int j = 0; j < 8; ++j) acc[j] = f32x4{0.f, 0.f, 0.f, 0.f};
#pragma unroll
  for (int t = 0; t < 4; ++t) {
#pragma unroll
    for (int j = 0; j < 8; ++j) acc[j] = mfma(colfrag(st + 40 + 8 * t, j), pf[t], acc[j]);
    PIN();
#pragma unroll
    for (int i = 0; i < 8; ++i) issue(40 + P_W + 8 * t + i);
    PIN();
  }
  STAMP(7);
  if (l16 < 8) {
    LAS float* pr = (LAS float*)(L + L_PART) + (w * 8 + l16) * PSTR + 32 * g4;
#pragma unroll
    for (int r = 0; r < 4; ++r) {
      *(LAS f32x4*)(pr + 8 * r) = f32x4{acc[0][r], acc[1][r], acc[2][r], acc[3][r]};
      *(LAS f32x4*)(pr + 8 * r + 4) = f32x4{acc[4][r], acc[5][r], acc[6][r], acc[7][r]};
    }
  }
  __syncthreads();
  {
    const f32x2 ml = *(const LAS f32x2*)(L + L_ML + ((lane & 7) * 8 + w) * 8);
    float M = ml[0];
    M = fmaxf(M, dpp<XOR1>(M)); M = fmaxf(M, dpp<XOR2>(M)); M = fmaxf(M, dpp<XOR7>(M));
    const float sc = __builtin_amdgcn_exp2f(ml[0] - M);
    float Ls = sc * ml[1];
    Ls += dpp<XOR1>(Ls); Ls += dpp<XOR2>(Ls); Ls += dpp<XOR7>(Ls);
    f32x2 o = {0.f, 0.f};
#pragma unroll
    for (int i = 0; i < 8; ++i) {
      const float sci = asf(__builtin_amdgcn_readlane(asu(sc), i));
      o += sci * *(const LAS f32x2*)((const LAS float*)(L + L_PART) + (i * 8 + w) * PSTR + 2 * lane);
    }
    const int row = ((br * 32 + kvh * 8 + w) * 4 + b) * 8 + s;
    stc2(u32x2{pkbf(o[0], o[1]), tag}, rWS, lane * 8, WS_OD + row * 512);
    if (lane == 0) stc4(u32x4{asu(M), tag, asu(Ls), tag}, rWS, 0, WS_OS + row * 16);
  }
  STAMP(8);

  if (w < 4) {
    const int row0 = ((br * 32 + hO) * 4 + w) * 8;
    const int dV = (lane >> 5) * 512 + (lane & 31) * 16;
    u32x4 d[4], sv;
    for (unsigned spin = 0; spin < kSpin; ++spin) {
      sv = ldc4(rWS, (lane & 7) * 16, WS_OS + row0 * 16);
#pragma unroll
      for (int j = 0; j < 4; ++j) d[j] = ldc4(rWS, dV, WS_OD + row0 * 512 + j * 1024);
      bool ok = sv[1] == tag && sv[3] == tag;
#pragma unroll
      for (int j = 0; j < 4; ++j) ok = ok && d[j][1] == tag && d[j][3] == tag;
      if (__all(ok)) break;
      __builtin_amdgcn_s_sleep(1);
    }
    STAMP(9);
    float M = asf(sv[0]);
    M = fmaxf(M, dpp<XOR1>(M)); M = fmaxf(M, dpp<XOR2>(M)); M = fmaxf(M, dpp<XOR7>(M));
    const float sc = __builtin_amdgcn_exp2f(asf(sv[0]) - M);
    float Ls = sc * asf(sv[2]);
    Ls += dpp<XOR1>(Ls); Ls += dpp<XOR2>(Ls); Ls += dpp<XOR7>(Ls);
    const float cf = sc / Ls;
    f32x4 o = {0.f, 0.f, 0.f, 0.f};
#pragma unroll
    for (int j = 0; j < 4; ++j) {
      const float c0 = asf(__builtin_amdgcn_readlane(asu(cf), 2 * j)), c1 = asf(__builtin_amdgcn_readlane(asu(cf), 2 * j + 1));
      const float cj = lane < 32 ? c0 : c1;
      o += cj * f32x4{bflo(d[j][0]), bfhi(d[j][0]), bflo(d[j][2]), bfhi(d[j][2])};
    }
#pragma unroll
    for (int e = 0; e < 4; ++e) o[e] = x32(o[e], false);
    if (lane < 32) *(LAS u32x2*)(L + L_OB + w * 256 + lane * 8) = u32x2{pkbf(o[0], o[1]), pkbf(o[2], o[3])};
  }
  __syncthreads();
  {
    const u32x4 of = *(const LAS u32x4*)(L + L_OB + (l16 & 3) * 256 + (32 * (w >> 1) + 8 * g4) * 2);
    f32x4 oa[8];
#pragma unroll
    for (int j = 0; j < 8; ++j) oa[j] = mfma(colfrag(st + 72, j), of, f32x4{0.f, 0.f, 0.f, 0.f});
    if (l16 < 4) {
      LAS float* red = (LAS float*)(L + L_RED) + ((w >> 1) * 4 + l16) * 256 + 128 * (w & 1) + 32 * g4;
#pragma unroll
      for (int r = 0; r < 4; ++r) {
        *(LAS f32x4*)(red + 8 * r) = f32x4{oa[0][r], oa[1][r], oa[2][r], oa[3][r]};
        *(LAS f32x4*)(red + 8 * r + 4) = f32x4{oa[4][r], oa[5][r], oa[6][r], oa[7][r]};
      }
    }
  }
  __syncthreads();
  {
    const int bb = tid >> 7, cp = tid & 127;
    const LAS f32x2* red = (const LAS f32x2*)(L + L_RED) + bb * 128 + cp;
    const f32x2 o = (red[0] + red[512]) + (red[1024] + red[1536]);
    const int r = (bb * 4 + ns) * 16 + (cp >> 3);
    stc4(u32x4{asu(o[0]), tag, asu(o[1]), tag}, rWS, ((r * 64) * 16 + 2 * (cp & 7)) * 8, WS_F + ((br * 32 + hO) * 16) * 8);
  }
  STAMP(10);

  if (w == 0) {
    u32x4 d[8];
    for (unsigned spin = 0; spin < kSpin; ++spin) {
#pragma unroll
      for (int j = 0; j < 8; ++j) d[j] = ldc4(rWS, lane * 16, WS_F + c * 8192 + j * 1024);
      bool ok = true;
#pragma unroll
      for (int j = 0; j < 8; ++j) ok = ok && d[j][1] == tag && d[j][3] == tag;
      if (__all(ok)) break;
      __builtin_amdgcn_s_sleep(1);
    }
    STAMP(11);
    float a0 = (asf(d[0][0]) + asf(d[1][0])) + (asf(d[2][0]) + asf(d[3][0]));
    float a1 = (asf(d[0][2]) + asf(d[1][2])) + (asf(d[2][2]) + asf(d[3][2]));
    float b0 = (asf(d[4][0]) + asf(d[5][0])) + (asf(d[6][0]) + asf(d[7][0]));
    float b1 = (asf(d[4][2]) + asf(d[5][2])) + (asf(d[6][2]) + asf(d[7][2]));
    a0 = x32(x16(a0 + dpp<XOR8>(a0), false), false);
    a1 = x32(x16(a1 + dpp<XOR8>(a1), false), false);
    b0 = x32(x16(b0 + dpp<XOR8>(b0), false), false);
    b1 = x32(x16(b1 + dpp<XOR8>(b1), false), false);
    const int bb = c >> 6;
    float z = 0.f;
#pragma unroll
    for (int i = 0; i < 8; ++i) z += ((const LAS float*)(L + L_GP))[i * 4 + bb];
    z = rbf(z);
    const float gt = rbf(1.f / (1.f + __expf(-z)));
    const float gn = rbf(1.f - gt);
    const float o0 = rbf(gt * rbf(a0)) + rbf(gn * rbf(b0));
    const float o1 = rbf(gt * rbf(a1)) + rbf(gn * rbf(b1));
    if (lane < 8) ((u32*)p.out)[bb * 512 + ((c >> 4) & 3) * 128 + (c & 15) * 8 + lane] = pkbf(o0, o1);
    if (c == 0 && lane == 0) __builtin_amdgcn_raw_buffer_store_b32(tag, rWS, 0, WS_EPOCH, AUX_SC1);
    STAMP(12);
  }
}

extern "C" void kernel_launch(void* const* d_in, const int* in_sizes, int n_in, void* d_out, int out_size, void* d_ws, size_t ws_size,
                              hipStream_t stream) {
  Params p;
  p.x = d_in[0]; p.gw = d_in[10];
  p.k0 = d_in[1]; p.v0 = d_in[2]; p.wq0 = d_in[3]; p.wo0 = d_in[4];
  p.k1 = d_in[6]; p.v1 = d_in[7]; p.wq1 = d_in[8]; p.wo1 = d_in[9];
  p.out = d_out; p.ws = d_ws;
  hipLaunchKernelGGL(gated_mix_decode_kernel, dim3(256), dim3(512), 0, stream, p);
}
```

```cpp
#include <hip/hip_runtime.h>
#include <hip/hip_bf16.h>
#include <stdint.h>

typedef unsigned short u16;
typedef unsigned int u32;
typedef unsigned long long u64;
typedef u32 u32x2 __attribute__((ext_vector_type(2)));
typedef u32 u32x4 __attribute__((ext_vector_type(4)));
typedef float f32x2 __attribute__((ext_vector_type(2)));
typedef float f32x4 __attribute__((ext_vector_type(4)));
typedef short bf16x8 __attribute__((ext_vector_type(8)));
typedef __amdgpu_buffer_rsrc_t rsrc_t;
#define LAS __attribute__((address_space(3)))

#define P_D1 0
#define P_W 32
#define P_POLL 16
#define P_PACE 6

constexpr int KV_BYTES = 4 * 4 * 8192 * 128 * 2;
constexpr int W_BYTES = 1024 * 4096 * 2;
constexpr int AUX_NT = 2, AUX_SC1 = 16;

constexpr int WS_EPOCH = 0;
constexpr int WS_Q = 4096;
constexpr int WS_OD = WS_Q + 2 * 4 * 4 * 2048 * 8;
constexpr int WS_OS = WS_OD + 2 * 32 * 4 * 8 * 64 * 8;
constexpr int WS_F = WS_OS + 2 * 32 * 4 * 8 * 16;
constexpr int WS_END = WS_F + 256 * 64 * 16 * 8;
constexpr int WS_STAMP = 8 << 20;

constexpr int L_KST = 0;
constexpr int L_RED = L_KST + 8 * 8192;
constexpr int L_QS = L_RED + 16384;
constexpr int PSTR = 132;
constexpr int L_PART = L_QS + 2048;
constexpr int L_ML = L_PART + 8 * 8 * PSTR * 4;
constexpr int L_OB = L_ML + 512;
constexpr int L_GP = L_OB + 1024;
constexpr int L_END = L_GP + 128;

struct Params {
  const void* x; const void* gw;
  const void* k0; const void* v0; const void* wq0; const void* wo0;
  const void* k1; const void* v1; const void* wq1; const void* wo1;
  void* out; void* ws;
};

__device__ __forceinline__ rsrc_t mkr(const void* p, int bytes) { return __builtin_amdgcn_make_buffer_rsrc((void*)p, 0, bytes, 0x00020000); }
__device__ __forceinline__ u32x4 ldnt(rsrc_t r, int voff, int soff) { return __builtin_amdgcn_raw_buffer_load_b128(r, voff, soff, AUX_NT); }
__device__ __forceinline__ u32x4 ldpl(rsrc_t r, int voff, int soff) { return __builtin_amdgcn_raw_buffer_load_b128(r, voff, soff, 0); }
__device__ __forceinline__ u32x4 ldc4(rsrc_t r, int voff, int soff) { return __builtin_amdgcn_raw_buffer_load_b128(r, voff, soff, AUX_SC1); }
__device__ __forceinline__ u32x2 ldc2(rsrc_t r, int voff, int soff) { return __builtin_amdgcn_raw_buffer_load_b64(r, voff, soff, AUX_SC1); }
__device__ __forceinline__ void stc4(u32x4 v, rsrc_t r, int voff, int soff) { __builtin_amdgcn_raw_buffer_store_b128(v, r, voff, soff, AUX_SC1); }
__device__ __forceinline__ void stc2(u32x2 v, rsrc_t r, int voff, int soff) { __builtin_amdgcn_raw_buffer_store_b64(v, r, voff, soff, AUX_SC1); }
__device__ __forceinline__ f32x4 mfma(u32x4 a, u32x4 b, f32x4 c) {
  return __builtin_amdgcn_mfma_f32_16x16x32_bf16(__builtin_bit_cast(bf16x8, a), __builtin_bit_cast(bf16x8, b), c, 0, 0, 0);
}
__device__ __forceinline__ float asf(u32 u) { return __builtin_bit_cast(float, u); }
__device__ __forceinline__ u32 asu(float f) { return __builtin_bit_cast(u32, f); }
typedef __bf16 bf2_t __attribute__((ext_vector_type(2)));
__device__ __forceinline__ u32 pkbf(float lo, float hi) {
  const f32x2 v = {lo, hi};
  return __builtin_bit_cast(u32, __builtin_convertvector(v, bf2_t));
}
__device__ __forceinline__ float rbf(float f) { return asf(pkbf(f, 0.f) << 16); }
__device__ __forceinline__ float bflo(u32 u) { return asf(u << 16); }
__device__ __forceinline__ float bfhi(u32 u) { return asf(u & 0xffff0000u); }
__device__ __forceinline__ float x16(float v, bool mx) {
  auto s = __builtin_amdgcn_permlane16_swap(asu(v), asu(v), false, false);
  return mx ? fmaxf(asf(s[0]), asf(s[1])) : asf(s[0]) + asf(s[1]);
}
__device__ __forceinline__ float x32(float v, bool mx) {
  auto s = __builtin_amdgcn_permlane32_swap(asu(v), asu(v), false, false);
  return mx ? fmaxf(asf(s[0]), asf(s[1])) : asf(s[0]) + asf(s[1]);
}
template <int CTRL> __device__ __forceinline__ float dpp(float v) {
  return asf(__builtin_amdgcn_mov_dpp(asu(v), CTRL, 0xf, 0xf, true));
}
constexpr int XOR1 = 0xB1, XOR2 = 0x4E, XOR7 = 0x141, XOR8 = 0x128;
#define SB() __builtin_amdgcn_sched_barrier(0)
#define PIN() do { asm volatile("" ::: "memory"); __builtin_amdgcn_sched_barrier(0); } while (0)
template <int N> __device__ __forceinline__ void waitvm() { asm volatile("s_waitcnt vmcnt(%0)" ::"n"(N) : "memory"); }
constexpr unsigned kSpin = 1u << 18;

__device__ __forceinline__ u32x4 colfrag(const u32x4* v, int j) {
  const u32 sel = (j & 1) ? 0x07060302u : 0x05040100u;
  const int d = j >> 1;
  u32x4 f;
  f[0] = __builtin_amdgcn_perm(v[1][d], v[0][d], sel);
  f[1] = __builtin_amdgcn_perm(v[3][d], v[2][d], sel);
  f[2] = __builtin_amdgcn_perm(v[5][d], v[4][d], sel);
  f[3] = __builtin_amdgcn_perm(v[7][d], v[6][d], sel);
  return f;
}

#define STAMP(i) do { } while (0)
#define STAMP_FLUSH() do { } while (0)

__global__ void __launch_bounds__(512, 2) gated_mix_decode_kernel(Params p) {
  __shared__ __attribute__((aligned(16))) unsigned char smem[L_END];
  LAS unsigned char* L = (LAS unsigned char*)smem;
  const int tid = threadIdx.x, lane = tid & 63, w = __builtin_amdgcn_readfirstlane(tid >> 6);
  const int c = blockIdx.x, g = c & 31, s = c >> 5, br = g >> 4, b = (g >> 2) & 3, kvh = g & 3, m = b * 8 + s;
  const int g4 = lane >> 4, l16 = lane & 15;
  const rsrc_t rK = mkr(br ? p.k1 : p.k0, KV_BYTES), rV = mkr(br ? p.v1 : p.v0, KV_BYTES);
  const rsrc_t rWQ = mkr(br ? p.wq1 : p.wq0, W_BYTES), rWO = mkr(br ? p.wo1 : p.wo0, W_BYTES);
  const rsrc_t rX = mkr(p.x, 4 * 1024 * 2), rG = mkr(p.gw, 1024 * 2), rWS = mkr(p.ws, WS_END);
  STAMP(0);

  const int kvS = (b * 4 + kvh) * (8192 * 256) + (s * 256 + w * 4) * 1024;
  const int kvV = lane * 16;
  const int wqV = (32 * ((lane >> 3) & 1) + 8 * g4) * 8192 + (lane & 7) * 16;
  const int wqS = (512 * (m & 1) + 64 * w) * 8192 + (kvh * 1024 + 64 * (m >> 1)) * 2;
  const int hO = kvh * 8 + (m >> 2), ns = m & 3;
  const int woV = (8 * g4) * 2048 + l16 * 16;
  const int woS = (hO * 128 + 32 * (w >> 1)) * 2048 + (256 * ns + 128 * (w & 1)) * 2;
  u32x4 st[80];
  auto issue = [&](int n) {
    if (n < 8) st[n] = ldnt(rWQ, wqV, wqS + n * 8192);
    else if (n < 40) { const int j = n - 8; st[n] = ldnt(rK, kvV, kvS + ((j >> 2) * 32 + (j & 3)) * 1024); }
    else if (n < 72) { const int j = n - 40; st[n] = ldnt(rV, kvV, kvS + ((j >> 2) * 32 + (j & 3)) * 1024); }
    else if (n < 80) st[n] = ldnt(rWO, woV, woS + (n - 72) * 2048);
  };

  const u32 epoch = __builtin_amdgcn_raw_buffer_load_b32(rWS, 0, WS_EPOCH, AUX_SC1);
  const int xk = 512 * (m & 1) + 64 * w + 32 * ((l16 >> 2) & 1) + 8 * g4;
  const u32x4 xf = ldpl(rX, ((lane & 3) * 1024 + xk) * 2, 0);
  const int gk = 128 * w + 32 * (l16 >> 2) + 8 * g4;
  const u32x4 xg = ldpl(rX, ((lane & 3) * 1024 + gk) * 2, 0);
  const u32x4 gf = ldpl(rG, gk * 2, 0);
  PIN();
#pragma unroll
  for (int n = 0; n < 8 + P_D1; ++n) { issue(n); PIN(); }

  {
    f32x4 qa[8];
#pragma unroll
    for (int j = 0; j < 8; ++j) qa[j] = mfma(xf, colfrag(st, j), f32x4{0.f, 0.f, 0.f, 0.f});
    STAMP(1);
    if (g4 == ((lane >> 3) & 1) && g4 < 2) {
      LAS float* red = (LAS float*)(L + L_RED) + ((w * 2 + g4) * 4) * 64 + (lane & 7) * 8;
#pragma unroll
      for (int r = 0; r < 4; ++r) {
        *(LAS f32x4*)(red + r * 64) = f32x4{qa[0][r], qa[1][r], qa[2][r], qa[3][r]};
        *(LAS f32x4*)(red + r * 64 + 4) = f32x4{qa[4][r], qa[5][r], qa[6][r], qa[7][r]};
      }
    }
    float gs = 0.f;
#pragma unroll
    for (int d = 0; d < 4; ++d) gs += rbf(bflo(xg[d]) * bflo(gf[d])) + rbf(bfhi(xg[d]) * bfhi(gf[d]));
    gs += dpp<0x124>(gs);
    gs += dpp<XOR8>(gs);
    gs = x16(gs, false);
    gs = x32(gs, false);
    if (lane < 4) ((LAS float*)(L + L_GP))[w * 4 + lane] = gs;
  }
  const u32 tag = epoch + 1u;
  __syncthreads();
  if (w < 4) {
    const LAS float* red = (const LAS float*)(L + L_RED) + w * 64 + lane;
    float a = 0.f;
#pragma unroll
    for (int i = 0; i < 16; ++i) a += red[i * 256];
    stc2(u32x2{asu(a), tag}, rWS, lane * 8, WS_Q + ((((br * 4 + kvh) * 4 + w) * 2 + (m & 1)) * 1024 + (m >> 1) * 64) * 8);
    STAMP(2);
  }
  PIN();

  const int qS = WS_Q + (((br * 4 + kvh) * 4 + b) * 2048 + w * 128) * 8;
#pragma unroll
  for (int n = 8 + P_D1; n < 8 + P_POLL; ++n) { if (P_PACE) waitvm<(P_PACE ? P_PACE - 1 : 0)>(); issue(n); PIN(); }
  u32x4 qg0 = ldc4(rWS, lane * 16, qS), qg1 = ldc4(rWS, lane * 16, qS + 8192);
  PIN();
#pragma unroll
  for (int n = 8 + P_POLL; n < 8 + P_W; ++n) { issue(n); PIN(); }
  if (!__all(qg0[1] == tag && qg0[3] == tag && qg1[1] == tag && qg1[3] == tag)) {
    for (unsigned spin = 0; spin < kSpin; ++spin) {
      __builtin_amdgcn_s_sleep(2);
      qg0 = ldc4(rWS, lane * 16, qS);
      qg1 = ldc4(rWS, lane * 16, qS + 8192);
      if (__all(qg0[1] == tag && qg0[3] == tag && qg1[1] == tag && qg1[3] == tag)) break;
    }
  }
  STAMP(3);
  ((LAS u32*)(L + L_QS))[tid] = pkbf(asf(qg0[0]) + asf(qg1[0]), asf(qg0[2]) + asf(qg1[2]));
  __syncthreads();
  u32x4 qf[4];
#pragma unroll
  for (int kk = 0; kk < 4; ++kk) qf[kk] = *(const LAS u32x4*)(L + L_QS + (l16 & 7) * 256 + kk * 64 + g4 * 16);
  STAMP(4);

  f32x4 S[8];
  {
    LAS unsigned char* kst = L + L_KST + w * 8192;
#pragma unroll
    for (int T = 0; T < 8; ++T) {
      LAS unsigned char* kb = kst + (T & 1) * 4096;
#pragma unroll
      for (int i = 0; i < 4; ++i) {
        const int rho = 4 * g4 + i;
        *(LAS u32x4*)(kb + rho * 256 + ((l16 ^ rho) * 16)) = st[8 + 4 * T + i];
      }
      PIN();
#pragma unroll
      for (int i = 0; i < 4; ++i) issue(8 + P_W + 4 * T + i);
      PIN();
      __builtin_amdgcn_fence(__ATOMIC_RELEASE, "wavefront");
      __builtin_amdgcn_wave_barrier();
      __builtin_amdgcn_fence(__ATOMIC_ACQUIRE, "wavefront");
      f32x4 a = {0.f, 0.f, 0.f, 0.f};
#pragma unroll
      for (int kk = 0; kk < 4; ++kk) {
        const u32x4 kf = *(const LAS u32x4*)(kb + l16 * 256 + (((4 * kk + g4) ^ l16) * 16));
        a = mfma(kf, qf[kk], a);
      }
      S[T] = a;
    }
  }
  STAMP(5);
  constexpr float SC = 0.08838834764831845f * 1.4426950408889634f;
  float mx = S[0][0];
#pragma unroll
  for (int T = 0; T < 8; ++T)
#pragma unroll
    for (int r = 0; r < 4; ++r) mx = fmaxf(mx, S[T][r]);
  mx = x32(x16(mx, true), true);
  const float mxs = mx * SC;
  float ls = 0.f;
  u32x4 pf[4];
#pragma unroll
  for (int T = 0; T < 8; ++T) {
    float e[4];
#pragma unroll
    for (int r = 0; r < 4; ++r) { e[r] = __builtin_amdgcn_exp2f(S[T][r] * SC - mxs); ls += e[r]; }
    pf[T >> 1][(T & 1) * 2] = pkbf(e[0], e[1]);
    pf[T >> 1][(T & 1) * 2 + 1] = pkbf(e[2], e[3]);
  }
  ls = x32(x16(ls, false), false);
  if (lane < 8) *(LAS f32x2*)(L + L_ML + (w * 8 + lane) * 8) = f32x2{mxs, ls};
  STAMP(6);
  f32x4 acc[8];
#pragma unroll
  for (int j = 0; j < 8; ++j) acc[j] = f32x4{0.f, 0.f, 0.f, 0.f};
#pragma unroll
  for (int t = 0; t < 4; ++t) {
#pragma unroll
    for (int j = 0; j < 8; ++j) acc[j] = mfma(colfrag(st + 40 + 8 * t, j), pf[t], acc[j]);
    PIN();
#pragma unroll
    for (int i = 0; i < 8; ++i) issue(40 + P_W + 8 * t + i);
    PIN();
  }
  STAMP(7);
  if (l16 < 8) {
    LAS float* pr = (LAS float*)(L + L_PART) + (w * 8 + l16) * PSTR + 32 * g4;
#pragma unroll
    for (int r = 0; r < 4; ++r) {
      *(LAS f32x4*)(pr + 8 * r) = f32x4{acc[0][r], acc[1][r], acc[2][r], acc[3][r]};
      *(LAS f32x4*)(pr + 8 * r + 4) = f32x4{acc[4][r], acc[5][r], acc[6][r], acc[7][r]};
    }
  }
  __syncthreads();
  {
    const f32x2 ml = *(const LAS f32x2*)(L + L_ML + ((lane & 7) * 8 + w) * 8);
    float M = ml[0];
    M = fmaxf(M, dpp<XOR1>(M)); M = fmaxf(M, dpp<XOR2>(M)); M = fmaxf(M, dpp<XOR7>(M));
    const float sc = __builtin_amdgcn_exp2f(ml[0] - M);
    float Ls = sc * ml[1];
    Ls += dpp<XOR1>(Ls); Ls += dpp<XOR2>(Ls); Ls += dpp<XOR7>(Ls);
    f32x2 o = {0.f, 0.f};
#pragma unroll
    for (int i = 0; i < 8; ++i) {
      const float sci = asf(__builtin_amdgcn_readlane(asu(sc), i));
      o += sci * *(const LAS f32x2*)((const LAS float*)(L + L_PART) + (i * 8 + w) * PSTR + 2 * lane);
    }
    const int row = ((br * 32 + kvh * 8 + w) * 4 + b) * 8 + s;
    stc2(u32x2{pkbf(o[0], o[1]), tag}, rWS, lane * 8, WS_OD + row * 512);
    if (lane == 0) stc4(u32x4{asu(M), tag, asu(Ls), tag}, rWS, 0, WS_OS + row * 16);
  }
  STAMP(8);

  if (w < 4) {
    const int row0 = ((br * 32 + hO) * 4 + w) * 8;
    const int dV = (lane >> 5) * 512 + (lane & 31) * 16;
    u32x4 d[4], sv;
    for (unsigned spin = 0; spin < kSpin; ++spin) {
      sv = ldc4(rWS, (lane & 7) * 16, WS_OS + row0 * 16);
#pragma unroll
      for (int j = 0; j < 4; ++j) d[j] = ldc4(rWS, dV, WS_OD + row0 * 512 + j * 1024);
      bool ok = sv[1] == tag && sv[3] == tag;
#pragma unroll
      for (int j = 0; j < 4; ++j) ok = ok && d[j][1] == tag && d[j][3] == tag;
      if (__all(ok)) break;
      __builtin_amdgcn_s_sleep(1);
    }
    STAMP(9);
    float M = asf(sv[0]);
    M = fmaxf(M, dpp<XOR1>(M)); M = fmaxf(M, dpp<XOR2>(M)); M = fmaxf(M, dpp<XOR7>(M));
    const float sc = __builtin_amdgcn_exp2f(asf(sv[0]) - M);
    float Ls = sc * asf(sv[2]);
    Ls += dpp<XOR1>(Ls); Ls += dpp<XOR2>(Ls); Ls += dpp<XOR7>(Ls);
    const float cf = sc / Ls;
    f32x4 o = {0.f, 0.f, 0.f, 0.f};
#pragma unroll
    for (int j = 0; j < 4; ++j) {
      const float c0 = asf(__builtin_amdgcn_readlane(asu(cf), 2 * j)), c1 = asf(__builtin_amdgcn_readlane(asu(cf), 2 * j + 1));
      const float cj = lane < 32 ? c0 : c1;
      o += cj * f32x4{bflo(d[j][0]), bfhi(d[j][0]), bflo(d[j][2]), bfhi(d[j][2])};
    }
#pragma unroll
    for (int e = 0; e < 4; ++e) o[e] = x32(o[e], false);
    if (lane < 32) *(LAS u32x2*)(L + L_OB + w * 256 + lane * 8) = u32x2{pkbf(o[0], o[1]), pkbf(o[2], o[3])};
  }
  __syncthreads();
  {
    const u32x4 of = *(const LAS u32x4*)(L + L_OB + (l16 & 3) * 256 + (32 * (w >> 1) + 8 * g4) * 2);
    f32x4 oa[8];
#pragma unroll
    for (int j = 0; j < 8; ++j) oa[j] = mfma(colfrag(st + 72, j), of, f32x4{0.f, 0.f, 0.f, 0.f});
    if (l16 < 4) {
      LAS float* red = (LAS float*)(L + L_RED) + ((w >> 1) * 4 + l16) * 256 + 128 * (w & 1) + 32 * g4;
#pragma unroll
      for (int r = 0; r < 4; ++r) {
        *(LAS f32x4*)(red + 8 * r) = f32x4{oa[0][r], oa[1][r], oa[2][r], oa[3][r]};
        *(LAS f32x4*)(red + 8 * r + 4) = f32x4{oa[4][r], oa[5][r], oa[6][r], oa[7][r]};
      }
    }
  }
  __syncthreads();
  {
    const int bb = tid >> 7, cp = tid & 127;
    const LAS f32x2* red = (const LAS f32x2*)(L + L_RED) + bb * 128 + cp;
    const f32x2 o = (red[0] + red[512]) + (red[1024] + red[1536]);
    const int r = (bb * 4 + ns) * 16 + (cp >> 3);
    stc4(u32x4{asu(o[0]), tag, asu(o[1]), tag}, rWS, ((r * 64) * 16 + 2 * (cp & 7)) * 8, WS_F + ((br * 32 + hO) * 16) * 8);
  }
  STAMP(10);

  if (w == 0) {
    u32x4 d[8];
    for (unsigned spin = 0; spin < kSpin; ++spin) {
#pragma unroll
      for (int j = 0; j < 8; ++j) d[j] = ldc4(rWS, lane * 16, WS_F + c * 8192 + j * 1024);
      bool ok = true;
#pragma unroll
      for (int j = 0; j < 8; ++j) ok = ok && d[j][1] == tag && d[j][3] == tag;
      if (__all(ok)) break;
      __builtin_amdgcn_s_sleep(1);
    }
    STAMP(11);
    float a0 = (asf(d[0][0]) + asf(d[1][0])) + (asf(d[2][0]) + asf(d[3][0]));
    float a1 = (asf(d[0][2]) + asf(d[1][2])) + (asf(d[2][2]) + asf(d[3][2]));
    float b0 = (asf(d[4][0]) + asf(d[5][0])) + (asf(d[6][0]) + asf(d[7][0]));
    float b1 = (asf(d[4][2]) + asf(d[5][2])) + (asf(d[6][2]) + asf(d[7][2]));
    a0 = x32(x16(a0 + dpp<XOR8>(a0), false), false);
    a1 = x32(x16(a1 + dpp<XOR8>(a1), false), false);
    b0 = x32(x16(b0 + dpp<XOR8>(b0), false), false);
    b1 = x32(x16(b1 + dpp<XOR8>(b1), false), false);
    const int bb = c >> 6;
    float z = 0.f;
#pragma unroll
    for (int i = 0; i < 8; ++i) z += ((const LAS float*)(L + L_GP))[i * 4 + bb];
    z = rbf(z);
    const float gt = rbf(1.f / (1.f + __expf(-z)));
    const float gn = rbf(1.f - gt);
    const float o0 = rbf(gt * rbf(a0)) + rbf(gn * rbf(b0));
    const float o1 = rbf(gt * rbf(a1)) + rbf(gn * rbf(b1));
    if (lane < 8) ((u32*)p.out)[bb * 512 + ((c >> 4) & 3) * 128 + (c & 15) * 8 + lane] = pkbf(o0, o1);
    if (c == 0 && lane == 0) __builtin_amdgcn_raw_buffer_store_b32(tag, rWS, 0, WS_EPOCH, AUX_SC1);
    STAMP(12);
  }
  STAMP_FLUSH();
}

extern "C" void kernel_launch(void* const* d_in, const int* in_sizes, int n_in, void* d_out, int out_size, void* d_ws, size_t ws_size,
                              hipStream_t stream) {
  Params p;
  p.x = d_in[0]; p.gw = d_in[10];
  p.k0 = d_in[1]; p.v0 = d_in[2]; p.wq0 = d_in[3]; p.wo0 = d_in[4];
  p.k1 = d_in[6]; p.v1 = d_in[7]; p.wq1 = d_in[8]; p.wo1 = d_in[9];
  p.out = d_out; p.ws = d_ws;
  hipLaunchKernelGGL(gated_mix_decode_kernel, dim3(256), dim3(512), 0, stream, p);
}
```

```cpp
#include <hip/hip_runtime.h>
#include <hip/hip_bf16.h>
#include <stdint.h>

typedef unsigned short u16;
typedef unsigned int u32;
typedef unsigned long long u64;
typedef u32 u32x2 __attribute__((ext_vector_type(2)));
typedef u32 u32x4 __attribute__((ext_vector_type(4)));
typedef float f32x2 __attribute__((ext_vector_type(2)));
typedef float f32x4 __attribute__((ext_vector_type(4)));
typedef short bf16x8 __attribute__((ext_vector_type(8)));
typedef __amdgpu_buffer_rsrc_t rsrc_t;
#define LAS __attribute__((address_space(3)))

#define P_D1 0
#define P_W 32
#define P_POLL 16
#define P_PACE 6

constexpr int KV_BYTES = 4 * 4 * 8192 * 128 * 2;
constexpr int W_BYTES = 1024 * 4096 * 2;
constexpr int AUX_NT = 2, AUX_SC1 = 16;

constexpr int WS_EPOCH = 0;
constexpr int WS_Q = 4096;
constexpr int WS_OD = WS_Q + 2 * 4 * 4 * 2048 * 8;
constexpr int WS_OS = WS_OD + 2 * 32 * 4 * 8 * 64 * 8;
constexpr int WS_F = WS_OS + 2 * 32 * 4 * 8 * 16;
constexpr int WS_END = WS_F + 256 * 64 * 16 * 8;
constexpr int WS_STAMP = 8 << 20;

constexpr int L_KST = 0;
constexpr int L_RED = L_KST + 8 * 8192;
constexpr int L_QS = L_RED + 16384;
constexpr int PSTR = 132;
constexpr int L_PART = L_QS + 2048;
constexpr int L_ML = L_PART + 8 * 8 * PSTR * 4;
constexpr int L_OB = L_ML + 512;
constexpr int L_GP = L_OB + 1024;
constexpr int L_END = L_GP + 128;

struct Params {
  const void* x; const void* gw;
  const void* k0; const void* v0; const void* wq0; const void* wo0;
  const void* k1; const void* v1; const void* wq1; const void* wo1;
  void* out; void* ws;
};

__device__ __forceinline__ rsrc_t mkr(const void* p, int bytes) { return __builtin_amdgcn_make_buffer_rsrc((void*)p, 0, bytes, 0x00020000); }
template <int AUX> __device__ __forceinline__ u32x4 ldntT(rsrc_t r, int voff, int soff) { return __builtin_amdgcn_raw_buffer_load_b128(r, voff, soff, AUX); }
#define ldnt(r, v, s_) ldntT<AUXS>(r, v, s_)
__device__ __forceinline__ u32x4 ldpl(rsrc_t r, int voff, int soff) { return __builtin_amdgcn_raw_buffer_load_b128(r, voff, soff, 0); }
__device__ __forceinline__ u32x4 ldc4(rsrc_t r, int voff, int soff) { return __builtin_amdgcn_raw_buffer_load_b128(r, voff, soff, AUX_SC1); }
__device__ __forceinline__ u32x2 ldc2(rsrc_t r, int voff, int soff) { return __builtin_amdgcn_raw_buffer_load_b64(r, voff, soff, AUX_SC1); }
__device__ __forceinline__ void stc4(u32x4 v, rsrc_t r, int voff, int soff) { __builtin_amdgcn_raw_buffer_store_b128(v, r, voff, soff, AUX_SC1); }
__device__ __forceinline__ void stc2(u32x2 v, rsrc_t r, int voff, int soff) { __builtin_amdgcn_raw_buffer_store_b64(v, r, voff, soff, AUX_SC1); }
__device__ __forceinline__ f32x4 mfma(u32x4 a, u32x4 b, f32x4 c) {
  return __builtin_amdgcn_mfma_f32_16x16x32_bf16(__builtin_bit_cast(bf16x8, a), __builtin_bit_cast(bf16x8, b), c, 0, 0, 0);
}
__device__ __forceinline__ float asf(u32 u) { return __builtin_bit_cast(float, u); }
__device__ __forceinline__ u32 asu(float f) { return __builtin_bit_cast(u32, f); }
typedef __bf16 bf2_t __attribute__((ext_vector_type(2)));
__device__ __forceinline__ u32 pkbf(float lo, float hi) {
  const f32x2 v = {lo, hi};
  return __builtin_bit_cast(u32, __builtin_convertvector(v, bf2_t));
}
__device__ __forceinline__ float rbf(float f) { return asf(pkbf(f, 0.f) << 16); }
__device__ __forceinline__ float bflo(u32 u) { return asf(u << 16); }
__device__ __forceinline__ float bfhi(u32 u) { return asf(u & 0xffff0000u); }
__device__ __forceinline__ float x16(float v, bool mx) {
  auto s = __builtin_amdgcn_permlane16_swap(asu(v), asu(v), false, false);
  return mx ? fmaxf(asf(s[0]), asf(s[1])) : asf(s[0]) + asf(s[1]);
}
__device__ __forceinline__ float x32(float v, bool mx) {
  auto s = __builtin_amdgcn_permlane32_swap(asu(v), asu(v), false, false);
  return mx ? fmaxf(asf(s[0]), asf(s[1])) : asf(s[0]) + asf(s[1]);
}
template <int CTRL> __device__ __forceinline__ float dpp(float v) {
  return asf(__builtin_amdgcn_mov_dpp(asu(v), CTRL, 0xf, 0xf, true));
}
constexpr int XOR1 = 0xB1, XOR2 = 0x4E, XOR7 = 0x141, XOR8 = 0x128;
#define SB() __builtin_amdgcn_sched_barrier(0)
#define PIN() do { asm volatile("" ::: "memory"); __builtin_amdgcn_sched_barrier(0); } while (0)
template <int N> __device__ __forceinline__ void waitvm() { asm volatile("s_waitcnt vmcnt(%0)" ::"n"(N) : "memory"); }
constexpr unsigned kSpin = 1u << 18;

__device__ __forceinline__ u32x4 colfrag(const u32x4* v, int j) {
  const u32 sel = (j & 1) ? 0x07060302u : 0x05040100u;
  const int d = j >> 1;
  u32x4 f;
  f[0] = __builtin_amdgcn_perm(v[1][d], v[0][d], sel);
  f[1] = __builtin_amdgcn_perm(v[3][d], v[2][d], sel);
  f[2] = __builtin_amdgcn_perm(v[5][d], v[4][d], sel);
  f[3] = __builtin_amdgcn_perm(v[7][d], v[6][d], sel);
  return f;
}

#define STAMP(i) do { } while (0)
#define STAMP_FLUSH() do { } while (0)

template <int AUXS> __device__ __forceinline__ void body(const Params& p, LAS unsigned char* L) {
  const int tid = threadIdx.x, lane = tid & 63, w = __builtin_amdgcn_readfirstlane(tid >> 6);
  const int c = blockIdx.x, g = c & 31, s = c >> 5, br = g >> 4, b = (g >> 2) & 3, kvh = g & 3, m = b * 8 + s;
  const int g4 = lane >> 4, l16 = lane & 15;
  const rsrc_t rK = mkr(br ? p.k1 : p.k0, KV_BYTES), rV = mkr(br ? p.v1 : p.v0, KV_BYTES);
  const rsrc_t rWQ = mkr(br ? p.wq1 : p.wq0, W_BYTES), rWO = mkr(br ? p.wo1 : p.wo0, W_BYTES);
  const rsrc_t rX = mkr(p.x, 4 * 1024 * 2), rG = mkr(p.gw, 1024 * 2), rWS = mkr(p.ws, WS_END);
  STAMP(0);

  const int kvS = (b * 4 + kvh) * (8192 * 256) + (s * 256 + w * 4) * 1024;
  const int kvV = lane * 16;
  const int wqV = (32 * ((lane >> 3) & 1) + 8 * g4) * 8192 + (lane & 7) * 16;
  const int wqS = (512 * (m & 1) + 64 * w) * 8192 + (kvh * 1024 + 64 * (m >> 1)) * 2;
  const int hO = kvh * 8 + (m >> 2), ns = m & 3;
  const int woV = (8 * g4) * 2048 + l16 * 16;
  const int woS = (hO * 128 + 32 * (w >> 1)) * 2048 + (256 * ns + 128 * (w & 1)) * 2;
  u32x4 st[80];
  auto issue = [&](int n) {
    if (n < 8) st[n] = ldnt(rWQ, wqV, wqS + n * 8192);
    else if (n < 40) { const int j = n - 8; st[n] = ldnt(rK, kvV, kvS + ((j >> 2) * 32 + (j & 3)) * 1024); }
    else if (n < 72) { const int j = n - 40; st[n] = ldnt(rV, kvV, kvS + ((j >> 2) * 32 + (j & 3)) * 1024); }
    else if (n < 80) st[n] = ldnt(rWO, woV, woS + (n - 72) * 2048);
  };

  const u32 epoch = __builtin_amdgcn_raw_buffer_load_b32(rWS, 0, WS_EPOCH, AUX_SC1);
  const int xk = 512 * (m & 1) + 64 * w + 32 * ((l16 >> 2) & 1) + 8 * g4;
  const u32x4 xf = ldpl(rX, ((lane & 3) * 1024 + xk) * 2, 0);
  const int gk = 128 * w + 32 * (l16 >> 2) + 8 * g4;
  const u32x4 xg = ldpl(rX, ((lane & 3) * 1024 + gk) * 2, 0);
  const u32x4 gf = ldpl(rG, gk * 2, 0);
  PIN();
#pragma unroll
  for (int n = 0; n < 8 + P_D1; ++n) { issue(n); PIN(); }

  {
    f32x4 qa[8];
#pragma unroll
    for (int j = 0; j < 8; ++j) qa[j] = mfma(xf, colfrag(st, j), f32x4{0.f, 0.f, 0.f, 0.f});
    STAMP(1);
    if (g4 == ((lane >> 3) & 1) && g4 < 2) {
      LAS float* red = (LAS float*)(L + L_RED) + ((w * 2 + g4) * 4) * 64 + (lane & 7) * 8;
#pragma unroll
      for (int r = 0; r < 4; ++r) {
        *(LAS f32x4*)(red + r * 64) = f32x4{qa[0][r], qa[1][r], qa[2][r], qa[3][r]};
        *(LAS f32x4*)(red + r * 64 + 4) = f32x4{qa[4][r], qa[5][r], qa[6][r], qa[7][r]};
      }
    }
    float gs = 0.f;
#pragma unroll
    for (int d = 0; d < 4; ++d) gs += rbf(bflo(xg[d]) * bflo(gf[d])) + rbf(bfhi(xg[d]) * bfhi(gf[d]));
    gs += dpp<0x124>(gs);
    gs += dpp<XOR8>(gs);
    gs = x16(gs, false);
    gs = x32(gs, false);
    if (lane < 4) ((LAS float*)(L + L_GP))[w * 4 + lane] = gs;
  }
  const u32 tag = epoch + 1u;
  __syncthreads();
  if (w < 4) {
    const LAS float* red = (const LAS float*)(L + L_RED) + w * 64 + lane;
    float a = 0.f;
#pragma unroll
    for (int i = 0; i < 16; ++i) a += red[i * 256];
    stc2(u32x2{asu(a), tag}, rWS, lane * 8, WS_Q + ((((br * 4 + kvh) * 4 + w) * 2 + (m & 1)) * 1024 + (m >> 1) * 64) * 8);
    STAMP(2);
  }
  PIN();

  const int qS = WS_Q + (((br * 4 + kvh) * 4 + b) * 2048 + w * 128) * 8;
#pragma unroll
  for (int n = 8 + P_D1; n < 8 + P_POLL; ++n) { if (P_PACE) waitvm<(P_PACE ? P_PACE - 1 : 0)>(); issue(n); PIN(); }
  u32x4 qg0 = ldc4(rWS, lane * 16, qS), qg1 = ldc4(rWS, lane * 16, qS + 8192);
  PIN();
#pragma unroll
  for (int n = 8 + P_POLL; n < 8 + P_W; ++n) { issue(n); PIN(); }
  if (!__all(qg0[1] == tag && qg0[3] == tag && qg1[1] == tag && qg1[3] == tag)) {
    for (unsigned spin = 0; spin < kSpin; ++spin) {
      __builtin_amdgcn_s_sleep(2);
      qg0 = ldc4(rWS, lane * 16, qS);
      qg1 = ldc4(rWS, lane * 16, qS + 8192);
      if (__all(qg0[1] == tag && qg0[3] == tag && qg1[1] == tag && qg1[3] == tag)) break;
    }
  }
  STAMP(3);
  ((LAS u32*)(L + L_QS))[tid] = pkbf(asf(qg0[0]) + asf(qg1[0]), asf(qg0[2]) + asf(qg1[2]));
  __syncthreads();
  u32x4 qf[4];
#pragma unroll
  for (int kk = 0; kk < 4; ++kk) qf[kk] = *(const LAS u32x4*)(L + L_QS + (l16 & 7) * 256 + kk * 64 + g4 * 16);
  STAMP(4);

  f32x4 S[8];
  {
    LAS unsigned char* kst = L + L_KST + w * 8192;
#pragma unroll
    for (int T = 0; T < 8; ++T) {
      LAS unsigned char* kb = kst + (T & 1) * 4096;
#pragma unroll
      for (int i = 0; i < 4; ++i) {
        const int rho = 4 * g4 + i;
        *(LAS u32x4*)(kb + rho * 256 + ((l16 ^ rho) * 16)) = st[8 + 4 * T + i];
      }
      PIN();
#pragma unroll
      for (int i = 0; i < 4; ++i) issue(8 + P_W + 4 * T + i);
      PIN();
      __builtin_amdgcn_fence(__ATOMIC_RELEASE, "wavefront");
      __builtin_amdgcn_wave_barrier();
      __builtin_amdgcn_fence(__ATOMIC_ACQUIRE, "wavefront");
      f32x4 a = {0.f, 0.f, 0.f, 0.f};
#pragma unroll
      for (int kk = 0; kk < 4; ++kk) {
        const u32x4 kf = *(const LAS u32x4*)(kb + l16 * 256 + (((4 * kk + g4) ^ l16) * 16));
        a = mfma(kf, qf[kk], a);
      }
      S[T] = a;
    }
  }
  STAMP(5);
  constexpr float SC = 0.08838834764831845f * 1.4426950408889634f;
  float mx = S[0][0];
#pragma unroll
  for (int T = 0; T < 8; ++T)
#pragma unroll
    for (int r = 0; r < 4; ++r) mx = fmaxf(mx, S[T][r]);
  mx = x32(x16(mx, true), true);
  const float mxs = mx * SC;
  float ls = 0.f;
  u32x4 pf[4];
#pragma unroll
  for (int T = 0; T < 8; ++T) {
    float e[4];
#pragma unroll
    for (int r = 0; r < 4; ++r) { e[r] = __builtin_amdgcn_exp2f(S[T][r] * SC - mxs); ls += e[r]; }
    pf[T >> 1][(T & 1) * 2] = pkbf(e[0], e[1]);
    pf[T >> 1][(T & 1) * 2 + 1] = pkbf(e[2], e[3]);
  }
  ls = x32(x16(ls, false), false);
  if (lane < 8) *(LAS f32x2*)(L + L_ML + (w * 8 + lane) * 8) = f32x2{mxs, ls};
  STAMP(6);
  f32x4 acc[8];
#pragma unroll
  for (int j = 0; j < 8; ++j) acc[j] = f32x4{0.f, 0.f, 0.f, 0.f};
#pragma unroll
  for (int t = 0; t < 4; ++t) {
#pragma unroll
    for (int j = 0; j < 8; ++j) acc[j] = mfma(colfrag(st + 40 + 8 * t, j), pf[t], acc[j]);
    PIN();
#pragma unroll
    for (int i = 0; i < 8; ++i) issue(40 + P_W + 8 * t + i);
    PIN();
  }
  STAMP(7);
  if (l16 < 8) {
    LAS float* pr = (LAS float*)(L + L_PART) + (w * 8 + l16) * PSTR + 32 * g4;
#pragma unroll
    for (int r = 0; r < 4; ++r) {
      *(LAS f32x4*)(pr + 8 * r) = f32x4{acc[0][r], acc[1][r], acc[2][r], acc[3][r]};
      *(LAS f32x4*)(pr + 8 * r + 4) = f32x4{acc[4][r], acc[5][r], acc[6][r], acc[7][r]};
    }
  }
  __syncthreads();
  {
    const f32x2 ml = *(const LAS f32x2*)(L + L_ML + ((lane & 7) * 8 + w) * 8);
    float M = ml[0];
    M = fmaxf(M, dpp<XOR1>(M)); M = fmaxf(M, dpp<XOR2>(M)); M = fmaxf(M, dpp<XOR7>(M));
    const float sc = __builtin_amdgcn_exp2f(ml[0] - M);
    float Ls = sc * ml[1];
    Ls += dpp<XOR1>(Ls); Ls += dpp<XOR2>(Ls); Ls += dpp<XOR7>(Ls);
    f32x2 o = {0.f, 0.f};
#pragma unroll
    for (int i = 0; i < 8; ++i) {
      const float sci = asf(__builtin_amdgcn_readlane(asu(sc), i));
      o += sci * *(const LAS f32x2*)((const LAS float*)(L + L_PART) + (i * 8 + w) * PSTR + 2 * lane);
    }
    const int row = ((br * 32 + kvh * 8 + w) * 4 + b) * 8 + s;
    stc2(u32x2{pkbf(o[0], o[1]), tag}, rWS, lane * 8, WS_OD + row * 512);
    if (lane == 0) stc4(u32x4{asu(M), tag, asu(Ls), tag}, rWS, 0, WS_OS + row * 16);
  }
  STAMP(8);

  if (w < 4) {
    const int row0 = ((br * 32 + hO) * 4 + w) * 8;
    const int dV = (lane >> 5) * 512 + (lane & 31) * 16;
    u32x4 d[4], sv;
    for (unsigned spin = 0; spin < kSpin; ++spin) {
      sv = ldc4(rWS, (lane & 7) * 16, WS_OS + row0 * 16);
#pragma unroll
      for (int j = 0; j < 4; ++j) d[j] = ldc4(rWS, dV, WS_OD + row0 * 512 + j * 1024);
      bool ok = sv[1] == tag && sv[3] == tag;
#pragma unroll
      for (int j = 0; j < 4; ++j) ok = ok && d[j][1] == tag && d[j][3] == tag;
      if (__all(ok)) break;
      __builtin_amdgcn_s_sleep(1);
    }
    STAMP(9);
    float M = asf(sv[0]);
    M = fmaxf(M, dpp<XOR1>(M)); M = fmaxf(M, dpp<XOR2>(M)); M = fmaxf(M, dpp<XOR7>(M));
    const float sc = __builtin_amdgcn_exp2f(asf(sv[0]) - M);
    float Ls = sc * asf(sv[2]);
    Ls += dpp<XOR1>(Ls); Ls += dpp<XOR2>(Ls); Ls += dpp<XOR7>(Ls);
    const float cf = sc / Ls;
    f32x4 o = {0.f, 0.f, 0.f, 0.f};
#pragma unroll
    for (int j = 0; j < 4; ++j) {
      const float c0 = asf(__builtin_amdgcn_readlane(asu(cf), 2 * j)), c1 = asf(__builtin_amdgcn_readlane(asu(cf), 2 * j + 1));
      const float cj = lane < 32 ? c0 : c1;
      o += cj * f32x4{bflo(d[j][0]), bfhi(d[j][0]), bflo(d[j][2]), bfhi(d[j][2])};
    }
#pragma unroll
    for (int e = 0; e < 4; ++e) o[e] = x32(o[e], false);
    if (lane < 32) *(LAS u32x2*)(L + L_OB + w * 256 + lane * 8) = u32x2{pkbf(o[0], o[1]), pkbf(o[2], o[3])};
  }
  __syncthreads();
  {
    const u32x4 of = *(const LAS u32x4*)(L + L_OB + (l16 & 3) * 256 + (32 * (w >> 1) + 8 * g4) * 2);
    f32x4 oa[8];
#pragma unroll
    for (int j = 0; j < 8; ++j) oa[j] = mfma(colfrag(st + 72, j), of, f32x4{0.f, 0.f, 0.f, 0.f});
    if (l16 < 4) {
      LAS float* red = (LAS float*)(L + L_RED) + ((w >> 1) * 4 + l16) * 256 + 128 * (w & 1) + 32 * g4;
#pragma unroll
      for (int r = 0; r < 4; ++r) {
        *(LAS f32x4*)(red + 8 * r) = f32x4{oa[0][r], oa[1][r], oa[2][r], oa[3][r]};
        *(LAS f32x4*)(red + 8 * r + 4) = f32x4{oa[4][r], oa[5][r], oa[6][r], oa[7][r]};
      }
    }
  }
  __syncthreads();
  {
    const int bb = tid >> 7, cp = tid & 127;
    const LAS f32x2* red = (const LAS f32x2*)(L + L_RED) + bb * 128 + cp;
    const f32x2 o = (red[0] + red[512]) + (red[1024] + red[1536]);
    const int r = (bb * 4 + ns) * 16 + (cp >> 3);
    stc4(u32x4{asu(o[0]), tag, asu(o[1]), tag}, rWS, ((r * 64) * 16 + 2 * (cp & 7)) * 8, WS_F + ((br * 32 + hO) * 16) * 8);
  }
  STAMP(10);

  if (w == 0) {
    u32x4 d[8];
    for (unsigned spin = 0; spin < kSpin; ++spin) {
#pragma unroll
      for (int j = 0; j < 8; ++j) d[j] = ldc4(rWS, lane * 16, WS_F + c * 8192 + j * 1024);
      bool ok = true;
#pragma unroll
      for (int j = 0; j < 8; ++j) ok = ok && d[j][1] == tag && d[j][3] == tag;
      if (__all(ok)) break;
      __builtin_amdgcn_s_sleep(1);
    }
    STAMP(11);
    float a0 = (asf(d[0][0]) + asf(d[1][0])) + (asf(d[2][0]) + asf(d[3][0]));
    float a1 = (asf(d[0][2]) + asf(d[1][2])) + (asf(d[2][2]) + asf(d[3][2]));
    float b0 = (asf(d[4][0]) + asf(d[5][0])) + (asf(d[6][0]) + asf(d[7][0]));
    float b1 = (asf(d[4][2]) + asf(d[5][2])) + (asf(d[6][2]) + asf(d[7][2]));
    a0 = x32(x16(a0 + dpp<XOR8>(a0), false), false);
    a1 = x32(x16(a1 + dpp<XOR8>(a1), false), false);
    b0 = x32(x16(b0 + dpp<XOR8>(b0), false), false);
    b1 = x32(x16(b1 + dpp<XOR8>(b1), false), false);
    const int bb = c >> 6;
    float z = 0.f;
#pragma unroll
    for (int i = 0; i < 8; ++i) z += ((const LAS float*)(L + L_GP))[i * 4 + bb];
    z = rbf(z);
    const float gt = rbf(1.f / (1.f + __expf(-z)));
    const float gn = rbf(1.f - gt);
    const float o0 = rbf(gt * rbf(a0)) + rbf(gn * rbf(b0));
    const float o1 = rbf(gt * rbf(a1)) + rbf(gn * rbf(b1));
    if (lane < 8) ((u32*)p.out)[bb * 512 + ((c >> 4) & 3) * 128 + (c & 15) * 8 + lane] = pkbf(o0, o1);
    if (c == 0 && lane == 0) __builtin_amdgcn_raw_buffer_store_b32(tag, rWS, 0, WS_EPOCH, AUX_SC1);
    STAMP(12);
  }
  STAMP_FLUSH();
}

__global__ void __launch_bounds__(512, 2) gated_mix_decode_kernel(Params p) {
  __shared__ __attribute__((aligned(16))) unsigned char smem[L_END];
  LAS unsigned char* L = (LAS unsigned char*)smem;
  if (((blockIdx.x & 31) >> 4) == 1) body<AUX_NT>(p, L); else body<0>(p, L);
}

extern "C" void kernel_launch(void* const* d_in, const int* in_sizes, int n_in, void* d_out, int out_size, void* d_ws, size_t ws_size,
                              hipStream_t stream) {
  Params p;
  p.x = d_in[0]; p.gw = d_in[10];
  p.k0 = d_in[1]; p.v0 = d_in[2]; p.wq0 = d_in[3]; p.wo0 = d_in[4];
  p.k1 = d_in[6]; p.v1 = d_in[7]; p.wq1 = d_in[8]; p.wo1 = d_in[9];
  p.out = d_out; p.ws = d_ws;
  hipLaunchKernelGGL(gated_mix_decode_kernel, dim3(256), dim3(512), 0, stream, p);
}
```

```cpp
#include <hip/hip_runtime.h>
#include <hip/hip_bf16.h>
#include <stdint.h>

typedef unsigned short u16;
typedef unsigned int u32;
typedef unsigned long long u64;
typedef u32 u32x2 __attribute__((ext_vector_type(2)));
typedef u32 u32x4 __attribute__((ext_vector_type(4)));
typedef float f32x2 __attribute__((ext_vector_type(2)));
typedef float f32x4 __attribute__((ext_vector_type(4)));
typedef short bf16x8 __attribute__((ext_vector_type(8)));
typedef __amdgpu_buffer_rsrc_t rsrc_t;
#define LAS __attribute__((address_space(3)))

#define P_D1 4
#define P_W 32
#define P_POLL 16
#define P_PACE 6

constexpr int KV_BYTES = 4 * 4 * 8192 * 128 * 2;
constexpr int W_BYTES = 1024 * 4096 * 2;
constexpr int AUX_NT = 2, AUX_SC1 = 16;

constexpr int WS_EPOCH = 0;
constexpr int WS_Q = 4096;
constexpr int WS_OD = WS_Q + 2 * 4 * 4 * 2048 * 8;
constexpr int WS_OS = WS_OD + 2 * 32 * 4 * 8 * 64 * 8;
constexpr int WS_F = WS_OS + 2 * 32 * 4 * 8 * 16;
constexpr int WS_END = WS_F + 256 * 64 * 16 * 8;
constexpr int WS_STAMP = 8 << 20;

constexpr int L_KST = 0;
constexpr int L_RED = L_KST + 8 * 8192;
constexpr int L_QS = L_RED + 16384;
constexpr int PSTR = 132;
constexpr int L_PART = L_QS + 2048;
constexpr int L_ML = L_PART + 8 * 8 * PSTR * 4;
constexpr int L_OB = L_ML + 512;
constexpr int L_GP = L_OB + 1024;
constexpr int L_END = L_GP + 128;

struct Params {
  const void* x; const void* gw;
  const void* k0; const void* v0; const void* wq0; const void* wo0;
  const void* k1; const void* v1; const void* wq1; const void* wo1;
  void* out; void* ws;
};

__device__ __forceinline__ rsrc_t mkr(const void* p, int bytes) { return __builtin_amdgcn_make_buffer_rsrc((void*)p, 0, bytes, 0x00020000); }
__device__ __forceinline__ u32x4 ldnt(rsrc_t r, int voff, int soff) { return __builtin_amdgcn_raw_buffer_load_b128(r, voff, soff, AUX_NT); }
__device__ __forceinline__ u32x4 ldpl(rsrc_t r, int voff, int soff) { return __builtin_amdgcn_raw_buffer_load_b128(r, voff, soff, 0); }
__device__ __forceinline__ u32x4 ldc4(rsrc_t r, int voff, int soff) { return __builtin_amdgcn_raw_buffer_load_b128(r, voff, soff, AUX_SC1); }
__device__ __forceinline__ u32x2 ldc2(rsrc_t r, int voff, int soff) { return __builtin_amdgcn_raw_buffer_load_b64(r, voff, soff, AUX_SC1); }
__device__ __forceinline__ void stc4(u32x4 v, rsrc_t r, int voff, int soff) { __builtin_amdgcn_raw_buffer_store_b128(v, r, voff, soff, AUX_SC1); }
__device__ __forceinline__ void stc2(u32x2 v, rsrc_t r, int voff, int soff) { __builtin_amdgcn_raw_buffer_store_b64(v, r, voff, soff, AUX_SC1); }
__device__ __forceinline__ f32x4 mfma(u32x4 a, u32x4 b, f32x4 c) {
  return __builtin_amdgcn_mfma_f32_16x16x32_bf16(__builtin_bit_cast(bf16x8, a), __builtin_bit_cast(bf16x8, b), c, 0, 0, 0);
}
__device__ __forceinline__ float asf(u32 u) { return __builtin_bit_cast(float, u); }
__device__ __forceinline__ u32 asu(float f) { return __builtin_bit_cast(u32, f); }
typedef __bf16 bf2_t __attribute__((ext_vector_type(2)));
__device__ __forceinline__ u32 pkbf(float lo, float hi) {
  const f32x2 v = {lo, hi};
  return __builtin_bit_cast(u32, __builtin_convertvector(v, bf2_t));
}
__device__ __forceinline__ float rbf(float f) { return asf(pkbf(f, 0.f) << 16); }
__device__ __forceinline__ float bflo(u32 u) { return asf(u << 16); }
__device__ __forceinline__ float bfhi(u32 u) { return asf(u & 0xffff0000u); }
__device__ __forceinline__ float x16(float v, bool mx) {
  auto s = __builtin_amdgcn_permlane16_swap(asu(v), asu(v), false, false);
  return mx ? fmaxf(asf(s[0]), asf(s[1])) : asf(s[0]) + asf(s[1]);
}
__device__ __forceinline__ float x32(float v, bool mx) {
  auto s = __builtin_amdgcn_permlane32_swap(asu(v), asu(v), false, false);
  return mx ? fmaxf(asf(s[0]), asf(s[1])) : asf(s[0]) + asf(s[1]);
}
template <int CTRL> __device__ __forceinline__ float dpp(float v) {
  return asf(__builtin_amdgcn_mov_dpp(asu(v), CTRL, 0xf, 0xf, true));
}
constexpr int XOR1 = 0xB1, XOR2 = 0x4E, XOR7 = 0x141, XOR8 = 0x128;
#define SB() __builtin_amdgcn_sched_barrier(0)
#define PIN() do { asm volatile("" ::: "memory"); __builtin_amdgcn_sched_barrier(0); } while (0)
template <int N> __device__ __forceinline__ void waitvm() { asm volatile("s_waitcnt vmcnt(%0)" ::"n"(N) : "memory"); }
constexpr unsigned kSpin = 1u << 18;

__device__ __forceinline__ u32x4 colfrag(const u32x4* v, int j) {
  const u32 sel = (j & 1) ? 0x07060302u : 0x05040100u;
  const int d = j >> 1;
  u32x4 f;
  f[0] = __builtin_amdgcn_perm(v[1][d], v[0][d], sel);
  f[1] = __builtin_amdgcn_perm(v[3][d], v[2][d], sel);
  f[2] = __builtin_amdgcn_perm(v[5][d], v[4][d], sel);
  f[3] = __builtin_amdgcn_perm(v[7][d], v[6][d], sel);
  return f;
}

#define STAMP(i) do { } while (0)
#define STAMP_FLUSH() do { } while (0)

__global__ void __launch_bounds__(512, 2) gated_mix_decode_kernel(Params p) {
  __shared__ __attribute__((aligned(16))) unsigned char smem[L_END];
  LAS unsigned char* L = (LAS unsigned char*)smem;
  const int tid = threadIdx.x, lane = tid & 63, w = __builtin_amdgcn_readfirstlane(tid >> 6);
  const int c = blockIdx.x, g = c & 31, s = c >> 5, br = g >> 4, b = (g >> 2) & 3, kvh = g & 3, m = b * 8 + s;
  const int g4 = lane >> 4, l16 = lane & 15;
  const rsrc_t rK = mkr(br ? p.k1 : p.k0, KV_BYTES), rV = mkr(br ? p.v1 : p.v0, KV_BYTES);
  const rsrc_t rWQ = mkr(br ? p.wq1 : p.wq0, W_BYTES), rWO = mkr(br ? p.wo1 : p.wo0, W_BYTES);
  const rsrc_t rX = mkr(p.x, 4 * 1024 * 2), rG = mkr(p.gw, 1024 * 2), rWS = mkr(p.ws, WS_END);
  STAMP(0);

  const int kvS = (b * 4 + kvh) * (8192 * 256) + (s * 256 + w * 4) * 1024;
  const int kvV = lane * 16;
  const int wqV = (32 * ((lane >> 3) & 1) + 8 * g4) * 8192 + (lane & 7) * 16;
  const int wqS = (512 * (m & 1) + 64 * w) * 8192 + (kvh * 1024 + 64 * (m >> 1)) * 2;
  const int hO = kvh * 8 + (m >> 2), ns = m & 3;
  const int woV = (8 * g4) * 2048 + l16 * 16;
  const int woS = (hO * 128 + 32 * (w >> 1)) * 2048 + (256 * ns + 128 * (w & 1)) * 2;
  u32x4 st[80];
  auto issue = [&](int n) {
    if (n < 8) st[n] = ldnt(rWQ, wqV, wqS + n * 8192);
    else if (n < 40) { const int j = n - 8; st[n] = ldnt(rK, kvV, kvS + ((j >> 2) * 32 + (j & 3)) * 1024); }
    else if (n < 72) { const int j = n - 40; st[n] = ldnt(rV, kvV, kvS + ((j >> 2) * 32 + (j & 3)) * 1024); }
    else if (n < 80) st[n] = ldnt(rWO, woV, woS + (n - 72) * 2048);
  };

  const u32 epoch = __builtin_amdgcn_raw_buffer_load_b32(rWS, 0, WS_EPOCH, AUX_SC1);
  const int xk = 512 * (m & 1) + 64 * w + 32 * ((l16 >> 2) & 1) + 8 * g4;
  const u32x4 xf = ldpl(rX, ((lane & 3) * 1024 + xk) * 2, 0);
  const int gk = 128 * w + 32 * (l16 >> 2) + 8 * g4;
  const u32x4 xg = ldpl(rX, ((lane & 3) * 1024 + gk) * 2, 0);
  const u32x4 gf = ldpl(rG, gk * 2, 0);
  PIN();
#pragma unroll
  for (int n = 0; n < 8 + P_D1; ++n) { issue(n); PIN(); }

  {
    f32x4 qa[8];
#pragma unroll
    for (int j = 0; j < 8; ++j) qa[j] = mfma(xf, colfrag(st, j), f32x4{0.f, 0.f, 0.f, 0.f});
    STAMP(1);
    if (g4 == ((lane >> 3) & 1) && g4 < 2) {
      LAS float* red = (LAS float*)(L + L_RED) + ((w * 2 + g4) * 4) * 64 + (lane & 7) * 8;
#pragma unroll
      for (int r = 0; r < 4; ++r) {
        *(LAS f32x4*)(red + r * 64) = f32x4{qa[0][r], qa[1][r], qa[2][r], qa[3][r]};
        *(LAS f32x4*)(red + r * 64 + 4) = f32x4{qa[4][r], qa[5][r], qa[6][r], qa[7][r]};
      }
    }
    float gs = 0.f;
#pragma unroll
    for (int d = 0; d < 4; ++d) gs += rbf(bflo(xg[d]) * bflo(gf[d])) + rbf(bfhi(xg[d]) * bfhi(gf[d]));
    gs += dpp<0x124>(gs);
    gs += dpp<XOR8>(gs);
    gs = x16(gs, false);
    gs = x32(gs, false);
    if (lane < 4) ((LAS float*)(L + L_GP))[w * 4 + lane] = gs;
  }
  const u32 tag = epoch + 1u;
  __syncthreads();
  if (w < 4) {
    const LAS float* red = (const LAS float*)(L + L_RED) + w * 64 + lane;
    float a = 0.f;
#pragma unroll
    for (int i = 0; i < 16; ++i) a += red[i * 256];
    stc2(u32x2{asu(a), tag}, rWS, lane * 8, WS_Q + ((((br * 4 + kvh) * 4 + w) * 2 + (m & 1)) * 1024 + (m >> 1) * 64) * 8);
    STAMP(2);
  }
  PIN();

  const int qS = WS_Q + (((br * 4 + kvh) * 4 + b) * 2048 + w * 128) * 8;
#pragma unroll
  for (int n = 8 + P_D1; n < 8 + P_POLL; ++n) { if (P_PACE) waitvm<(P_PACE ? P_PACE - 1 : 0)>(); issue(n); PIN(); }
  u32x4 qg0 = ldc4(rWS, lane * 16, qS), qg1 = ldc4(rWS, lane * 16, qS + 8192);
  PIN();
#pragma unroll
  for (int n = 8 + P_POLL; n < 8 + P_W; ++n) { issue(n); PIN(); }
  if (!__all(qg0[1] == tag && qg0[3] == tag && qg1[1] == tag && qg1[3] == tag)) {
    for (unsigned spin = 0; spin < kSpin; ++spin) {
      __builtin_amdgcn_s_sleep(2);
      qg0 = ldc4(rWS, lane * 16, qS);
      qg1 = ldc4(rWS, lane * 16, qS + 8192);
      if (__all(qg0[1] == tag && qg0[3] == tag && qg1[1] == tag && qg1[3] == tag)) break;
    }
  }
  STAMP(3);
  ((LAS u32*)(L + L_QS))[tid] = pkbf(asf(qg0[0]) + asf(qg1[0]), asf(qg0[2]) + asf(qg1[2]));
  __syncthreads();
  u32x4 qf[4];
#pragma unroll
  for (int kk = 0; kk < 4; ++kk) qf[kk] = *(const LAS u32x4*)(L + L_QS + (l16 & 7) * 256 + kk * 64 + g4 * 16);
  STAMP(4);

  f32x4 S[8];
  {
    LAS unsigned char* kst = L + L_KST + w * 8192;
#pragma unroll
    for (int T = 0; T < 8; ++T) {
      LAS unsigned char* kb = kst + (T & 1) * 4096;
#pragma unroll
      for (int i = 0; i < 4; ++i) {
        const int rho = 4 * g4 + i;
        *(LAS u32x4*)(kb + rho * 256 + ((l16 ^ rho) * 16)) = st[8 + 4 * T + i];
      }
      PIN();
#pragma unroll
      for (int i = 0; i < 4; ++i) issue(8 + P_W + 4 * T + i);
      PIN();
      __builtin_amdgcn_fence(__ATOMIC_RELEASE, "wavefront");
      __builtin_amdgcn_wave_barrier();
      __builtin_amdgcn_fence(__ATOMIC_ACQUIRE, "wavefront");
      f32x4 a = {0.f, 0.f, 0.f, 0.f};
#pragma unroll
      for (int kk = 0; kk < 4; ++kk) {
        const u32x4 kf = *(const LAS u32x4*)(kb + l16 * 256 + (((4 * kk + g4) ^ l16) * 16));
        a = mfma(kf, qf[kk], a);
      }
      S[T] = a;
    }
  }
  STAMP(5);
  constexpr float SC = 0.08838834764831845f * 1.4426950408889634f;
  float mx = S[0][0];
#pragma unroll
  for (int T = 0; T < 8; ++T)
#pragma unroll
    for (int r = 0; r < 4; ++r) mx = fmaxf(mx, S[T][r]);
  mx = x32(x16(mx, true), true);
  const float mxs = mx * SC;
  float ls = 0.f;
  u32x4 pf[4];
#pragma unroll
  for (int T = 0; T < 8; ++T) {
    float e[4];
#pragma unroll
    for (int r = 0; r < 4; ++r) { e[r] = __builtin_amdgcn_exp2f(S[T][r] * SC - mxs); ls += e[r]; }
    pf[T >> 1][(T & 1) * 2] = pkbf(e[0], e[1]);
    pf[T >> 1][(T & 1) * 2 + 1] = pkbf(e[2], e[3]);
  }
  ls = x32(x16(ls, false), false);
  if (lane < 8) *(LAS f32x2*)(L + L_ML + (w * 8 + lane) * 8) = f32x2{mxs, ls};
  STAMP(6);
  f32x4 acc[8];
#pragma unroll
  for (int j = 0; j < 8; ++j) acc[j] = f32x4{0.f, 0.f, 0.f, 0.f};
#pragma unroll
  for (int t = 0; t < 4; ++t) {
#pragma unroll
    for (int j = 0; j < 8; ++j) acc[j] = mfma(colfrag(st + 40 + 8 * t, j), pf[t], acc[j]);
    PIN();
#pragma unroll
    for (int i = 0; i < 8; ++i) issue(40 + P_W + 8 * t + i);
    PIN();
  }
  STAMP(7);
  if (l16 < 8) {
    LAS float* pr = (LAS float*)(L + L_PART) + (w * 8 + l16) * PSTR + 32 * g4;
#pragma unroll
    for (int r = 0; r < 4; ++r) {
      *(LAS f32x4*)(pr + 8 * r) = f32x4{acc[0][r], acc[1][r], acc[2][r], acc[3][r]};
      *(LAS f32x4*)(pr + 8 * r + 4) = f32x4{acc[4][r], acc[5][r], acc[6][r], acc[7][r]};
    }
  }
  __syncthreads();
  {
    const f32x2 ml = *(const LAS f32x2*)(L + L_ML + ((lane & 7) * 8 + w) * 8);
    float M = ml[0];
    M = fmaxf(M, dpp<XOR1>(M)); M = fmaxf(M, dpp<XOR2>(M)); M = fmaxf(M, dpp<XOR7>(M));
    const float sc = __builtin_amdgcn_exp2f(ml[0] - M);
    float Ls = sc * ml[1];
    Ls += dpp<XOR1>(Ls); Ls += dpp<XOR2>(Ls); Ls += dpp<XOR7>(Ls);
    f32x2 o = {0.f, 0.f};
#pragma unroll
    for (int i = 0; i < 8; ++i) {
      const float sci = asf(__builtin_amdgcn_readlane(asu(sc), i));
      o += sci * *(const LAS f32x2*)((const LAS float*)(L + L_PART) + (i * 8 + w) * PSTR + 2 * lane);
    }
    const int row = ((br * 32 + kvh * 8 + w) * 4 + b) * 8 + s;
    stc2(u32x2{pkbf(o[0], o[1]), tag}, rWS, lane * 8, WS_OD + row * 512);
    if (lane == 0) stc4(u32x4{asu(M), tag, asu(Ls), tag}, rWS, 0, WS_OS + row * 16);
  }
  STAMP(8);

  if (w < 4) {
    const int row0 = ((br * 32 + hO) * 4 + w) * 8;
    const int dV = (lane >> 5) * 512 + (lane & 31) * 16;
    u32x4 d[4], sv;
    for (unsigned spin = 0; spin < kSpin; ++spin) {
      sv = ldc4(rWS, (lane & 7) * 16, WS_OS + row0 * 16);
#pragma unroll
      for (int j = 0; j < 4; ++j) d[j] = ldc4(rWS, dV, WS_OD + row0 * 512 + j * 1024);
      bool ok = sv[1] == tag && sv[3] == tag;
#pragma unroll
      for (int j = 0; j < 4; ++j) ok = ok && d[j][1] == tag && d[j][3] == tag;
      if (__all(ok)) break;
      __builtin_amdgcn_s_sleep(1);
    }
    STAMP(9);
    float M = asf(sv[0]);
    M = fmaxf(M, dpp<XOR1>(M)); M = fmaxf(M, dpp<XOR2>(M)); M = fmaxf(M, dpp<XOR7>(M));
    const float sc = __builtin_amdgcn_exp2f(asf(sv[0]) - M);
    float Ls = sc * asf(sv[2]);
    Ls += dpp<XOR1>(Ls); Ls += dpp<XOR2>(Ls); Ls += dpp<XOR7>(Ls);
    const float cf = sc / Ls;
    f32x4 o = {0.f, 0.f, 0.f, 0.f};
#pragma unroll
    for (int j = 0; j < 4; ++j) {
      const float c0 = asf(__builtin_amdgcn_readlane(asu(cf), 2 * j)), c1 = asf(__builtin_amdgcn_readlane(asu(cf), 2 * j + 1));
      const float cj = lane < 32 ? c0 : c1;
      o += cj * f32x4{bflo(d[j][0]), bfhi(d[j][0]), bflo(d[j][2]), bfhi(d[j][2])};
    }
#pragma unroll
    for (int e = 0; e < 4; ++e) o[e] = x32(o[e], false);
    if (lane < 32) *(LAS u32x2*)(L + L_OB + w * 256 + lane * 8) = u32x2{pkbf(o[0], o[1]), pkbf(o[2], o[3])};
  }
  __syncthreads();
  {
    const u32x4 of = *(const LAS u32x4*)(L + L_OB + (l16 & 3) * 256 + (32 * (w >> 1) + 8 * g4) * 2);
    f32x4 oa[8];
#pragma unroll
    for (int j = 0; j < 8; ++j) oa[j] = mfma(colfrag(st + 72, j), of, f32x4{0.f, 0.f, 0.f, 0.f});
    if (l16 < 4) {
      LAS float* red = (LAS float*)(L + L_RED) + ((w >> 1) * 4 + l16) * 256 + 128 * (w & 1) + 32 * g4;
#pragma unroll
      for (int r = 0; r < 4; ++r) {
        *(LAS f32x4*)(red + 8 * r) = f32x4{oa[0][r], oa[1][r], oa[2][r], oa[3][r]};
        *(LAS f32x4*)(red + 8 * r + 4) = f32x4{oa[4][r], oa[5][r], oa[6][r], oa[7][r]};
      }
    }
  }
  __syncthreads();
  {
    const int bb = tid >> 7, cp = tid & 127;
    const LAS f32x2* red = (const LAS f32x2*)(L + L_RED) + bb * 128 + cp;
    const f32x2 o = (red[0] + red[512]) + (red[1024] + red[1536]);
    const int r = (bb * 4 + ns) * 16 + (cp >> 3);
    stc4(u32x4{asu(o[0]), tag, asu(o[1]), tag}, rWS, ((r * 64) * 16 + 2 * (cp & 7)) * 8, WS_F + ((br * 32 + hO) * 16) * 8);
  }
  STAMP(10);

  if (w == 0) {
    u32x4 d[8];
    for (unsigned spin = 0; spin < kSpin; ++spin) {
#pragma unroll
      for (int j = 0; j < 8; ++j) d[j] = ldc4(rWS, lane * 16, WS_F + c * 8192 + j * 1024);
      bool ok = true;
#pragma unroll
      for (int j = 0; j < 8; ++j) ok = ok && d[j][1] == tag && d[j][3] == tag;
      if (__all(ok)) break;
      __builtin_amdgcn_s_sleep(1);
    }
    STAMP(11);
    float a0 = (asf(d[0][0]) + asf(d[1][0])) + (asf(d[2][0]) + asf(d[3][0]));
    float a1 = (asf(d[0][2]) + asf(d[1][2])) + (asf(d[2][2]) + asf(d[3][2]));
    float b0 = (asf(d[4][0]) + asf(d[5][0])) + (asf(d[6][0]) + asf(d[7][0]));
    float b1 = (asf(d[4][2]) + asf(d[5][2])) + (asf(d[6][2]) + asf(d[7][2]));
    a0 = x32(x16(a0 + dpp<XOR8>(a0), false), false);
    a1 = x32(x16(a1 + dpp<XOR8>(a1), false), false);
    b0 = x32(x16(b0 + dpp<XOR8>(b0), false), false);
    b1 = x32(x16(b1 + dpp<XOR8>(b1), false), false);
    const int bb = c >> 6;
    float z = 0.f;
#pragma unroll
    for (int i = 0; i < 8; ++i) z += ((const LAS float*)(L + L_GP))[i * 4 + bb];
    z = rbf(z);
    const float gt = rbf(1.f / (1.f + __expf(-z)));
    const float gn = rbf(1.f - gt);
    const float o0 = rbf(gt * rbf(a0)) + rbf(gn * rbf(b0));
    const float o1 = rbf(gt * rbf(a1)) + rbf(gn * rbf(b1));
    if (lane < 8) ((u32*)p.out)[bb * 512 + ((c >> 4) & 3) * 128 + (c & 15) * 8 + lane] = pkbf(o0, o1);
    if (c == 0 && lane == 0) __builtin_amdgcn_raw_buffer_store_b32(tag, rWS, 0, WS_EPOCH, AUX_SC1);
    STAMP(12);
  }
  STAMP_FLUSH();
}

extern "C" void kernel_launch(void* const* d_in, const int* in_sizes, int n_in, void* d_out, int out_size, void* d_ws, size_t ws_size,
                              hipStream_t stream) {
  Params p;
  p.x = d_in[0]; p.gw = d_in[10];
  p.k0 = d_in[1]; p.v0 = d_in[2]; p.wq0 = d_in[3]; p.wo0 = d_in[4];
  p.k1 = d_in[6]; p.v1 = d_in[7]; p.wq1 = d_in[8]; p.wo1 = d_in[9];
  p.out = d_out; p.ws = d_ws;
  hipLaunchKernelGGL(gated_mix_decode_kernel, dim3(256), dim3(512), 0, stream, p);
}
```

```cpp
#define P_SL1 1
#define P_SL2 1
#include <hip/hip_runtime.h>
#include <hip/hip_bf16.h>
#include <stdint.h>

typedef unsigned short u16;
typedef unsigned int u32;
typedef unsigned long long u64;
typedef u32 u32x2 __attribute__((ext_vector_type(2)));
typedef u32 u32x4 __attribute__((ext_vector_type(4)));
typedef float f32x2 __attribute__((ext_vector_type(2)));
typedef float f32x4 __attribute__((ext_vector_type(4)));
typedef short bf16x8 __attribute__((ext_vector_type(8)));
typedef __amdgpu_buffer_rsrc_t rsrc_t;
#define LAS __attribute__((address_space(3)))

#define P_D1 4
#define P_W 32
#define P_POLL 16
#define P_PACE 6

constexpr int KV_BYTES = 4 * 4 * 8192 * 128 * 2;
constexpr int W_BYTES = 1024 * 4096 * 2;
constexpr int AUX_NT = 2, AUX_SC1 = 16;

constexpr int WS_EPOCH = 0;
constexpr int WS_Q = 4096;
constexpr int WS_OD = WS_Q + 2 * 4 * 4 * 2048 * 8;
constexpr int WS_OS = WS_OD + 2 * 32 * 4 * 8 * 64 * 8;
constexpr int WS_F = WS_OS + 2 * 32 * 4 * 8 * 16;
constexpr int WS_END = WS_F + 256 * 64 * 16 * 8;
constexpr int WS_STAMP = 8 << 20;

constexpr int L_KST = 0;
constexpr int L_RED = L_KST + 8 * 8192;
constexpr int L_QS = L_RED + 16384;
constexpr int PSTR = 132;
constexpr int L_PART = L_QS + 2048;
constexpr int L_ML = L_PART + 8 * 8 * PSTR * 4;
constexpr int L_OB = L_ML + 512;
constexpr int L_GP = L_OB + 1024;
constexpr int L_END = L_GP + 128;

struct Params {
  const void* x; const void* gw;
  const void* k0; const void* v0; const void* wq0; const void* wo0;
  const void* k1; const void* v1; const void* wq1; const void* wo1;
  void* out; void* ws;
};

__device__ __forceinline__ rsrc_t mkr(const void* p, int bytes) { return __builtin_amdgcn_make_buffer_rsrc((void*)p, 0, bytes, 0x00020000); }
__device__ __forceinline__ u32x4 ldnt(rsrc_t r, int voff, int soff) { return __builtin_amdgcn_raw_buffer_load_b128(r, voff, soff, AUX_NT); }
__device__ __forceinline__ u32x4 ldpl(rsrc_t r, int voff, int soff) { return __builtin_amdgcn_raw_buffer_load_b128(r, voff, soff, 0); }
__device__ __forceinline__ u32x4 ldc4(rsrc_t r, int voff, int soff) { return __builtin_amdgcn_raw_buffer_load_b128(r, voff, soff, AUX_SC1); }
__device__ __forceinline__ u32x2 ldc2(rsrc_t r, int voff, int soff) { return __builtin_amdgcn_raw_buffer_load_b64(r, voff, soff, AUX_SC1); }
__device__ __forceinline__ void stc4(u32x4 v, rsrc_t r, int voff, int soff) {
  __builtin_amdgcn_raw_buffer_store_b128(v, r, voff + soff, 0, AUX_SC1);
  __builtin_amdgcn_sched_barrier(0);
  asm volatile("s_nop 1");
  __builtin_amdgcn_sched_barrier(0);
}
__device__ __forceinline__ void stc2(u32x2 v, rsrc_t r, int voff, int soff) { __builtin_amdgcn_raw_buffer_store_b64(v, r, voff, soff, AUX_SC1); }
__device__ __forceinline__ f32x4 mfma(u32x4 a, u32x4 b, f32x4 c) {
  return __builtin_amdgcn_mfma_f32_16x16x32_bf16(__builtin_bit_cast(bf16x8, a), __builtin_bit_cast(bf16x8, b), c, 0, 0, 0);
}
__device__ __forceinline__ float asf(u32 u) { return __builtin_bit_cast(float, u); }
__device__ __forceinline__ u32 asu(float f) { return __builtin_bit_cast(u32, f); }
typedef __bf16 bf2_t __attribute__((ext_vector_type(2)));
__device__ __forceinline__ u32 pkbf(float lo, float hi) {
  const f32x2 v = {lo, hi};
  return __builtin_bit_cast(u32, __builtin_convertvector(v, bf2_t));
}
__device__ __forceinline__ float rbf(float f) { return asf(pkbf(f, 0.f) << 16); }
__device__ __forceinline__ float bflo(u32 u) { return asf(u << 16); }
__device__ __forceinline__ float bfhi(u32 u) { return asf(u & 0xffff0000u); }
__device__ __forceinline__ float x16(float v, bool mx) {
  auto s = __builtin_amdgcn_permlane16_swap(asu(v), asu(v), false, false);
  return mx ? fmaxf(asf(s[0]), asf(s[1])) : asf(s[0]) + asf(s[1]);
}
__device__ __forceinline__ float x32(float v, bool mx) {
  auto s = __builtin_amdgcn_permlane32_swap(asu(v), asu(v), false, false);
  return mx ? fmaxf(asf(s[0]), asf(s[1])) : asf(s[0]) + asf(s[1]);
}
template <int CTRL> __device__ __forceinline__ float dpp(float v) {
  return asf(__builtin_amdgcn_mov_dpp(asu(v), CTRL, 0xf, 0xf, true));
}
constexpr int XOR1 = 0xB1, XOR2 = 0x4E, XOR7 = 0x141, XOR8 = 0x128;
#define SB() __builtin_amdgcn_sched_barrier(0)
#define PIN() do { asm volatile("" ::: "memory"); __builtin_amdgcn_sched_barrier(0); } while (0)
template <int N> __device__ __forceinline__ void waitvm() { asm volatile("s_waitcnt vmcnt(%0)" ::"n"(N) : "memory"); }
constexpr unsigned kSpin = 1u << 18;

__device__ __forceinline__ u32x4 colfrag(const u32x4* v, int j) {
  const u32 sel = (j & 1) ? 0x07060302u : 0x05040100u;
  const int d = j >> 1;
  u32x4 f;
  f[0] = __builtin_amdgcn_perm(v[1][d], v[0][d], sel);
  f[1] = __builtin_amdgcn_perm(v[3][d], v[2][d], sel);
  f[2] = __builtin_amdgcn_perm(v[5][d], v[4][d], sel);
  f[3] = __builtin_amdgcn_perm(v[7][d], v[6][d], sel);
  return f;
}

#define STAMP(i) do { } while (0)
#define STAMP_FLUSH() do { } while (0)

__global__ void __launch_bounds__(512, 2) gated_mix_decode_kernel(Params p) {
  __shared__ __attribute__((aligned(16))) unsigned char smem[L_END];
  LAS unsigned char* L = (LAS unsigned char*)smem;
  const int tid = threadIdx.x, lane = tid & 63, w = __builtin_amdgcn_readfirstlane(tid >> 6);
  const int c = blockIdx.x, g = c & 31, s = c >> 5, br = g >> 4, b = (g >> 2) & 3, kvh = g & 3, m = b * 8 + s;
  const int g4 = lane >> 4, l16 = lane & 15;
  const rsrc_t rK = mkr(br ? p.k1 : p.k0, KV_BYTES), rV = mkr(br ? p.v1 : p.v0, KV_BYTES);
  const rsrc_t rWQ = mkr(br ? p.wq1 : p.wq0, W_BYTES), rWO = mkr(br ? p.wo1 : p.wo0, W_BYTES);
  const rsrc_t rX = mkr(p.x, 4 * 1024 * 2), rG = mkr(p.gw, 1024 * 2), rWS = mkr(p.ws, WS_END);
  STAMP(0);

  const int kvS = (b * 4 + kvh) * (8192 * 256) + (s * 256 + w * 4) * 1024;
  const int kvV = lane * 16;
  const int wqV = (32 * ((lane >> 3) & 1) + 8 * g4) * 8192 + (lane & 7) * 16;
  const int wqS = (512 * (m & 1) + 64 * w) * 8192 + (kvh * 1024 + 64 * (m >> 1)) * 2;
  const int hO = kvh * 8 + (m >> 2), ns = m & 3;
  const int woV = (8 * g4) * 2048 + l16 * 16;
  const int woS = (hO * 128 + 32 * (w >> 1)) * 2048 + (256 * ns + 128 * (w & 1)) * 2;
  u32x4 st[80];
  auto issue = [&](int n) {
    if (n < 8) st[n] = ldnt(rWQ, wqV, wqS + n * 8192);
    else if (n < 40) { const int j = n - 8; st[n] = ldnt(rK, kvV, kvS + ((j >> 2) * 32 + (j & 3)) * 1024); }
    else if (n < 72) { const int j = n - 40; st[n] = ldnt(rV, kvV, kvS + ((j >> 2) * 32 + (j & 3)) * 1024); }
    else if (n < 80) st[n] = ldnt(rWO, woV, woS + (n - 72) * 2048);
  };

  const u32 epoch = __builtin_amdgcn_raw_buffer_load_b32(rWS, 0, WS_EPOCH, AUX_SC1);
  const int xk = 512 * (m & 1) + 64 * w + 32 * ((l16 >> 2) & 1) + 8 * g4;
  const u32x4 xf = ldpl(rX, ((lane & 3) * 1024 + xk) * 2, 0);
  const int gk = 128 * w + 32 * (l16 >> 2) + 8 * g4;
  const u32x4 xg = ldpl(rX, ((lane & 3) * 1024 + gk) * 2, 0);
  const u32x4 gf = ldpl(rG, gk * 2, 0);
  PIN();
#pragma unroll
  for (int n = 0; n < 8 + P_D1; ++n) { issue(n); PIN(); }

  {
    f32x4 qa[8];
#pragma unroll
    for (int j = 0; j < 8; ++j) qa[j] = mfma(xf, colfrag(st, j), f32x4{0.f, 0.f, 0.f, 0.f});
    STAMP(1);
    if (g4 == ((lane >> 3) & 1) && g4 < 2) {
      LAS float* red = (LAS float*)(L + L_RED) + ((w * 2 + g4) * 4) * 64 + (lane & 7) * 8;
#pragma unroll
      for (int r = 0; r < 4; ++r) {
        *(LAS f32x4*)(red + r * 64) = f32x4{qa[0][r], qa[1][r], qa[2][r], qa[3][r]};
        *(LAS f32x4*)(red + r * 64 + 4) = f32x4{qa[4][r], qa[5][r], qa[6][r], qa[7][r]};
      }
    }
    float gs = 0.f;
#pragma unroll
    for (int d = 0; d < 4; ++d) gs += rbf(bflo(xg[d]) * bflo(gf[d])) + rbf(bfhi(xg[d]) * bfhi(gf[d]));
    gs += dpp<0x124>(gs);
    gs += dpp<XOR8>(gs);
    gs = x16(gs, false);
    gs = x32(gs, false);
    if (lane < 4) ((LAS float*)(L + L_GP))[w * 4 + lane] = gs;
  }
  const u32 tag = epoch + 1u;
  __syncthreads();
  if (w < 4) {
    const LAS float* red = (const LAS float*)(L + L_RED) + w * 64 + lane;
    float a = 0.f;
#pragma unroll
    for (int i = 0; i < 16; ++i) a += red[i * 256];
    stc2(u32x2{asu(a), tag}, rWS, lane * 8, WS_Q + ((((br * 4 + kvh) * 4 + w) * 2 + (m & 1)) * 1024 + (m >> 1) * 64) * 8);
    STAMP(2);
  }
  PIN();

  const int qS = WS_Q + (((br * 4 + kvh) * 4 + b) * 2048 + w * 128) * 8;
#pragma unroll
  for (int n = 8 + P_D1; n < 8 + P_POLL; ++n) { if (P_PACE) waitvm<(P_PACE ? P_PACE - 1 : 0)>(); issue(n); PIN(); }
  u32x4 qg0 = ldc4(rWS, lane * 16, qS), qg1 = ldc4(rWS, lane * 16, qS + 8192);
  PIN();
#pragma unroll
  for (int n = 8 + P_POLL; n < 8 + P_W; ++n) { issue(n); PIN(); }
  if (!__all(qg0[1] == tag && qg0[3] == tag && qg1[1] == tag && qg1[3] == tag)) {
    for (unsigned spin = 0; spin < kSpin; ++spin) {
      __builtin_amdgcn_s_sleep(2);
      qg0 = ldc4(rWS, lane * 16, qS);
      qg1 = ldc4(rWS, lane * 16, qS + 8192);
      if (__all(qg0[1] == tag && qg0[3] == tag && qg1[1] == tag && qg1[3] == tag)) break;
    }
  }
  STAMP(3);
  ((LAS u32*)(L + L_QS))[tid] = pkbf(asf(qg0[0]) + asf(qg1[0]), asf(qg0[2]) + asf(qg1[2]));
  __syncthreads();
  u32x4 qf[4];
#pragma unroll
  for (int kk = 0; kk < 4; ++kk) qf[kk] = *(const LAS u32x4*)(L + L_QS + (l16 & 7) * 256 + kk * 64 + g4 * 16);
  STAMP(4);

  f32x4 S[8];
  {
    LAS unsigned char* kst = L + L_KST + w * 8192;
#pragma unroll
    for (int T = 0; T < 8; ++T) {
      LAS unsigned char* kb = kst + (T & 1) * 4096;
#pragma unroll
      for (int i = 0; i < 4; ++i) {
        const int rho = 4 * g4 + i;
        *(LAS u32x4*)(kb + rho * 256 + ((l16 ^ rho) * 16)) = st[8 + 4 * T + i];
      }
      PIN();
#pragma unroll
      for (int i = 0; i < 4; ++i) issue(8 + P_W + 4 * T + i);
      PIN();
      __builtin_amdgcn_fence(__ATOMIC_RELEASE, "wavefront");
      __builtin_amdgcn_wave_barrier();
      __builtin_amdgcn_fence(__ATOMIC_ACQUIRE, "wavefront");
      f32x4 a = {0.f, 0.f, 0.f, 0.f};
#pragma unroll
      for (int kk = 0; kk < 4; ++kk) {
        const u32x4 kf = *(const LAS u32x4*)(kb + l16 * 256 + (((4 * kk + g4) ^ l16) * 16));
        a = mfma(kf, qf[kk], a);
      }
      S[T] = a;
    }
  }
  STAMP(5);
  constexpr float SC = 0.08838834764831845f * 1.4426950408889634f;
  float mx = S[0][0];
#pragma unroll
  for (int T = 0; T < 8; ++T)
#pragma unroll
    for (int r = 0; r < 4; ++r) mx = fmaxf(mx, S[T][r]);
  mx = x32(x16(mx, true), true);
  const float mxs = mx * SC;
  float ls = 0.f;
  u32x4 pf[4];
#pragma unroll
  for (int T = 0; T < 8; ++T) {
    float e[4];
#pragma unroll
    for (int r = 0; r < 4; ++r) { e[r] = __builtin_amdgcn_exp2f(S[T][r] * SC - mxs); ls += e[r]; }
    pf[T >> 1][(T & 1) * 2] = pkbf(e[0], e[1]);
    pf[T >> 1][(T & 1) * 2 + 1] = pkbf(e[2], e[3]);
  }
  ls = x32(x16(ls, false), false);
  if (lane < 8) *(LAS f32x2*)(L + L_ML + (w * 8 + lane) * 8) = f32x2{mxs, ls};
  STAMP(6);
  f32x4 acc[8];
#pragma unroll
  for (int j = 0; j < 8; ++j) acc[j] = f32x4{0.f, 0.f, 0.f, 0.f};
#pragma unroll
  for (int t = 0; t < 4; ++t) {
#pragma unroll
    for (int j = 0; j < 8; ++j) acc[j] = mfma(colfrag(st + 40 + 8 * t, j), pf[t], acc[j]);
    PIN();
#pragma unroll
    for (int i = 0; i < 8; ++i) issue(40 + P_W + 8 * t + i);
    PIN();
  }
  STAMP(7);
  if (l16 < 8) {
    LAS float* pr = (LAS float*)(L + L_PART) + (w * 8 + l16) * PSTR + 32 * g4;
#pragma unroll
    for (int r = 0; r < 4; ++r) {
      *(LAS f32x4*)(pr + 8 * r) = f32x4{acc[0][r], acc[1][r], acc[2][r], acc[3][r]};
      *(LAS f32x4*)(pr + 8 * r + 4) = f32x4{acc[4][r], acc[5][r], acc[6][r], acc[7][r]};
    }
  }
  __syncthreads();
  {
    const f32x2 ml = *(const LAS f32x2*)(L + L_ML + ((lane & 7) * 8 + w) * 8);
    float M = ml[0];
    M = fmaxf(M, dpp<XOR1>(M)); M = fmaxf(M, dpp<XOR2>(M)); M = fmaxf(M, dpp<XOR7>(M));
    const float sc = __builtin_amdgcn_exp2f(ml[0] - M);
    float Ls = sc * ml[1];
    Ls += dpp<XOR1>(Ls); Ls += dpp<XOR2>(Ls); Ls += dpp<XOR7>(Ls);
    f32x2 o = {0.f, 0.f};
#pragma unroll
    for (int i = 0; i < 8; ++i) {
      const float sci = asf(__builtin_amdgcn_readlane(asu(sc), i));
      o += sci * *(const LAS f32x2*)((const LAS float*)(L + L_PART) + (i * 8 + w) * PSTR + 2 * lane);
    }
    const int row = ((br * 32 + kvh * 8 + w) * 4 + b) * 8 + s;
    stc2(u32x2{pkbf(o[0], o[1]), tag}, rWS, lane * 8, WS_OD + row * 512);
    if (lane == 0) stc4(u32x4{asu(M), tag, asu(Ls), tag}, rWS, 0, WS_OS + row * 16);
  }
  STAMP(8);

  if (w < 4) {
    const int row0 = ((br * 32 + hO) * 4 + w) * 8;
    const int dV = (lane >> 5) * 512 + (lane & 31) * 16;
    u32x4 d[4], sv;
    for (unsigned spin = 0; spin < kSpin; ++spin) {
      sv = ldc4(rWS, (lane & 7) * 16, WS_OS + row0 * 16);
#pragma unroll
      for (int j = 0; j < 4; ++j) d[j] = ldc4(rWS, dV, WS_OD + row0 * 512 + j * 1024);
      bool ok = sv[1] == tag && sv[3] == tag;
#pragma unroll
      for (int j = 0; j < 4; ++j) ok = ok && d[j][1] == tag && d[j][3] == tag;
      if (__all(ok)) break;
      __builtin_amdgcn_s_sleep(P_SL1);
    }
    STAMP(9);
    float M = asf(sv[0]);
    M = fmaxf(M, dpp<XOR1>(M)); M = fmaxf(M, dpp<XOR2>(M)); M = fmaxf(M, dpp<XOR7>(M));
    const float sc = __builtin_amdgcn_exp2f(asf(sv[0]) - M);
    float Ls = sc * asf(sv[2]);
    Ls += dpp<XOR1>(Ls); Ls += dpp<XOR2>(Ls); Ls += dpp<XOR7>(Ls);
    const float cf = sc / Ls;
    f32x4 o = {0.f, 0.f, 0.f, 0.f};
#pragma unroll
    for (int j = 0; j < 4; ++j) {
      const float c0 = asf(__builtin_amdgcn_readlane(asu(cf), 2 * j)), c1 = asf(__builtin_amdgcn_readlane(asu(cf), 2 * j + 1));
      const float cj = lane < 32 ? c0 : c1;
      o += cj * f32x4{bflo(d[j][0]), bfhi(d[j][0]), bflo(d[j][2]), bfhi(d[j][2])};
    }
#pragma unroll
    for (int e = 0; e < 4; ++e) o[e] = x32(o[e], false);
    if (lane < 32) *(LAS u32x2*)(L + L_OB + w * 256 + lane * 8) = u32x2{pkbf(o[0], o[1]), pkbf(o[2], o[3])};
  }
  __syncthreads();
  {
    const u32x4 of = *(const LAS u32x4*)(L + L_OB + (l16 & 3) * 256 + (32 * (w >> 1) + 8 * g4) * 2);
    f32x4 oa[8];
#pragma unroll
    for (int j = 0; j < 8; ++j) oa[j] = mfma(colfrag(st + 72, j), of, f32x4{0.f, 0.f, 0.f, 0.f});
    if (l16 < 4) {
      LAS float* red = (LAS float*)(L + L_RED) + ((w >> 1) * 4 + l16) * 256 + 128 * (w & 1) + 32 * g4;
#pragma unroll
      for (int r = 0; r < 4; ++r) {
        *(LAS f32x4*)(red + 8 * r) = f32x4{oa[0][r], oa[1][r], oa[2][r], oa[3][r]};
        *(LAS f32x4*)(red + 8 * r + 4) = f32x4{oa[4][r], oa[5][r], oa[6][r], oa[7][r]};
      }
    }
  }
  __syncthreads();
  {
    const int bb = tid >> 7, cp = tid & 127;
    const LAS f32x2* red = (const LAS f32x2*)(L + L_RED) + bb * 128 + cp;
    const f32x2 o = (red[0] + red[512]) + (red[1024] + red[1536]);
    const int r = (bb * 4 + ns) * 16 + (cp >> 3);
    stc4(u32x4{asu(o[0]), tag, asu(o[1]), tag}, rWS, ((r * 64) * 16 + 2 * (cp & 7)) * 8, WS_F + ((br * 32 + hO) * 16) * 8);
  }
  STAMP(10);

  if (w == 0) {
    u32x4 d[8];
    for (unsigned spin = 0; spin < kSpin; ++spin) {
#pragma unroll
      for (int j = 0; j < 8; ++j) d[j] = ldc4(rWS, lane * 16, WS_F + c * 8192 + j * 1024);
      bool ok = true;
#pragma unroll
      for (int j = 0; j < 8; ++j) ok = ok && d[j][1] == tag && d[j][3] == tag;
      if (__all(ok)) break;
      __builtin_amdgcn_s_sleep(P_SL2);
    }
    STAMP(11);
    float a0 = (asf(d[0][0]) + asf(d[1][0])) + (asf(d[2][0]) + asf(d[3][0]));
    float a1 = (asf(d[0][2]) + asf(d[1][2])) + (asf(d[2][2]) + asf(d[3][2]));
    float b0 = (asf(d[4][0]) + asf(d[5][0])) + (asf(d[6][0]) + asf(d[7][0]));
    float b1 = (asf(d[4][2]) + asf(d[5][2])) + (asf(d[6][2]) + asf(d[7][2]));
    a0 = x32(x16(a0 + dpp<XOR8>(a0), false), false);
    a1 = x32(x16(a1 + dpp<XOR8>(a1), false), false);
    b0 = x32(x16(b0 + dpp<XOR8>(b0), false), false);
    b1 = x32(x16(b1 + dpp<XOR8>(b1), false), false);
    const int bb = c >> 6;
    float z = 0.f;
#pragma unroll
    for (int i = 0; i < 8; ++i) z += ((const LAS float*)(L + L_GP))[i * 4 + bb];
    z = rbf(z);
    const float gt = rbf(1.f / (1.f + __expf(-z)));
    const float gn = rbf(1.f - gt);
    const float o0 = rbf(gt * rbf(a0)) + rbf(gn * rbf(b0));
    const float o1 = rbf(gt * rbf(a1)) + rbf(gn * rbf(b1));
    if (lane < 8) ((u32*)p.out)[bb * 512 + ((c >> 4) & 3) * 128 + (c & 15) * 8 + lane] = pkbf(o0, o1);
    if (c == 0 && lane == 0) __builtin_amdgcn_raw_buffer_store_b32(tag, rWS, 0, WS_EPOCH, AUX_SC1);
    STAMP(12);
  }
  STAMP_FLUSH();
}

extern "C" void kernel_launch(void* const* d_in, const int* in_sizes, int n_in, void* d_out, int out_size, void* d_ws, size_t ws_size,
                              hipStream_t stream) {
  Params p;
  p.x = d_in[0]; p.gw = d_in[10];
  p.k0 = d_in[1]; p.v0 = d_in[2]; p.wq0 = d_in[3]; p.wo0 = d_in[4];
  p.k1 = d_in[6]; p.v1 = d_in[7]; p.wq1 = d_in[8]; p.wo1 = d_in[9];
  p.out = d_out; p.ws = d_ws;
  hipLaunchKernelGGL(gated_mix_decode_kernel, dim3(256), dim3(512), 0, stream, p);
}
```

```cpp
#include <hip/hip_runtime.h>
#include <hip/hip_bf16.h>
#include <stdint.h>

typedef unsigned short u16;
typedef unsigned int u32;
typedef unsigned long long u64;
typedef u32 u32x2 __attribute__((ext_vector_type(2)));
typedef u32 u32x4 __attribute__((ext_vector_type(4)));
typedef float f32x2 __attribute__((ext_vector_type(2)));
typedef float f32x4 __attribute__((ext_vector_type(4)));
typedef short bf16x8 __attribute__((ext_vector_type(8)));
typedef __amdgpu_buffer_rsrc_t rsrc_t;
#define LAS __attribute__((address_space(3)))

#define P_D1 4
#define P_PACE 6
#define P_POLL 16
#define F_AG 0
#define F_ROLE 2
#define F_RESC 0
#define P_NE 6
#define F_PP 0
#define P_PPS 3
#define P_TH1 32
#define P_TH2 16
#define P_SLP2 P_SLP
#define P_SLT 0
#define F_SKEW 0
#define F_SKEWX 0
#define P_LSL 2
#define P_SLP 4
#define P_MIMIC 1

constexpr int KV_BYTES = 4 * 4 * 8192 * 128 * 2;
constexpr int W_BYTES = 1024 * 4096 * 2;
constexpr int AUX_NT = 2, AUX_SC1 = 16;

constexpr int WS_EPOCH = 0;
constexpr int WS_Q = 4096;
constexpr int WS_OD = WS_Q + 2 * 4 * 4 * 2048 * 8;
constexpr int WS_OS = WS_OD + 2 * 32 * 4 * 8 * 64 * 8;
constexpr int WS_F = WS_OS + 2 * 32 * 4 * 8 * 16;
constexpr int WS_END = WS_F + 256 * 64 * 16 * 8;
constexpr int WS_STAMP = 8 << 20;

constexpr int L_KST = 0;
constexpr int L_RED = L_KST + 8 * 8192;
constexpr int L_QS = L_RED + 16640;
constexpr int PSTR = 132;
constexpr int L_PART = L_QS + 2048;
constexpr int L_ML = L_PART + 8 * 8 * PSTR * 4;
constexpr int L_OB = L_ML + 512;
constexpr int L_FIN = L_OB + 1024;
constexpr int L_GP = L_FIN + 512;
constexpr int L_CNT = L_GP + 128;
constexpr int L_END = L_CNT + 16;

struct Params {
  const void* x; const void* gw;
  const void* k0; const void* v0; const void* wq0; const void* wo0;
  const void* k1; const void* v1; const void* wq1; const void* wo1;
  void* out; void* ws;
};

__device__ __forceinline__ rsrc_t mkr(const void* p, int bytes) { return __builtin_amdgcn_make_buffer_rsrc((void*)p, 0, bytes, 0x00020000); }
__device__ __forceinline__ u32x4 ldnt(rsrc_t r, int voff, int soff) { return __builtin_amdgcn_raw_buffer_load_b128(r, voff, soff, AUX_NT); }
__device__ __forceinline__ u32x4 ldpl(rsrc_t r, int voff, int soff) { return __builtin_amdgcn_raw_buffer_load_b128(r, voff, soff, 0); }
__device__ __forceinline__ u32x4 ldc4(rsrc_t r, int voff, int soff) { return __builtin_amdgcn_raw_buffer_load_b128(r, voff, soff, AUX_SC1); }
__device__ __forceinline__ void stc4(u32x4 v, rsrc_t r, int voff, int soff) {
  __builtin_amdgcn_raw_buffer_store_b128(v, r, voff + soff, 0, AUX_SC1);
  __builtin_amdgcn_sched_barrier(0);
  asm volatile("s_nop 1");
  __builtin_amdgcn_sched_barrier(0);
}
__device__ __forceinline__ void stc2(u32x2 v, rsrc_t r, int voff, int soff) { __builtin_amdgcn_raw_buffer_store_b64(v, r, voff, soff, AUX_SC1); }
__device__ __forceinline__ f32x4 mfma(u32x4 a, u32x4 b, f32x4 c) {
  return __builtin_amdgcn_mfma_f32_16x16x32_bf16(__builtin_bit_cast(bf16x8, a), __builtin_bit_cast(bf16x8, b), c, 0, 0, 0);
}
__device__ __forceinline__ float asf(u32 u) { return __builtin_bit_cast(float, u); }
__device__ __forceinline__ u32 asu(float f) { return __builtin_bit_cast(u32, f); }
typedef __bf16 bf2_t __attribute__((ext_vector_type(2)));
__device__ __forceinline__ u32 pkbf(float lo, float hi) {
  const f32x2 v = {lo, hi};
  return __builtin_bit_cast(u32, __builtin_convertvector(v, bf2_t));
}
__device__ __forceinline__ float bflo(u32 u) { return asf(u << 16); }
__device__ __forceinline__ float bfhi(u32 u) { return asf(u & 0xffff0000u); }
__device__ __forceinline__ float rbf(float f) { return bflo(pkbf(f, 0.f)); }
__device__ __forceinline__ float x16(float v, bool mx) {
  auto s = __builtin_amdgcn_permlane16_swap(asu(v), asu(v), false, false);
  return mx ? fmaxf(asf(s[0]), asf(s[1])) : asf(s[0]) + asf(s[1]);
}
__device__ __forceinline__ float x32(float v, bool mx) {
  auto s = __builtin_amdgcn_permlane32_swap(asu(v), asu(v), false, false);
  return mx ? fmaxf(asf(s[0]), asf(s[1])) : asf(s[0]) + asf(s[1]);
}
template <int CTRL> __device__ __forceinline__ float dpp(float v) {
  return asf(__builtin_amdgcn_mov_dpp(asu(v), CTRL, 0xf, 0xf, true));
}
constexpr int ROR4 = 0x124, ROR8 = 0x128, XOR1 = 0xB1, XOR2 = 0x4E, XOR7 = 0x141;
template <int N> struct IC { static constexpr int value = N; };
#define SB() __builtin_amdgcn_sched_barrier(0)
#define PIN() do { asm volatile("" ::: "memory"); __builtin_amdgcn_sched_barrier(0); } while (0)
template <int N> __device__ __forceinline__ void waitvm() { asm volatile("s_waitcnt vmcnt(%0)" ::"n"(N) : "memory"); }
constexpr unsigned kSpin = 1u << 18;
__device__ __forceinline__ u32 lds_inc(LAS unsigned char* p) {
  u32 old = 0;
  if ((threadIdx.x & 63) == 0) old = __hip_atomic_fetch_add((LAS u32*)p, 1u, __ATOMIC_ACQ_REL, __HIP_MEMORY_SCOPE_WORKGROUP);
  return __builtin_amdgcn_readfirstlane(old);
}

__device__ __forceinline__ u32x4 colfrag(const u32x4* v, int j) {
  const u32 sel = (j & 1) ? 0x07060302u : 0x05040100u;
  const int d = j >> 1;
  u32x4 f;
  f[0] = __builtin_amdgcn_perm(v[1][d], v[0][d], sel);
  f[1] = __builtin_amdgcn_perm(v[3][d], v[2][d], sel);
  f[2] = __builtin_amdgcn_perm(v[5][d], v[4][d], sel);
  f[3] = __builtin_amdgcn_perm(v[7][d], v[6][d], sel);
  return f;
}

#define STAMP(i) do { } while (0)
#define STAMP_FLUSH() do { } while (0)

__global__ void __launch_bounds__(512, 2) gated_mix_decode_kernel(Params p) {
  __shared__ __attribute__((aligned(16))) unsigned char smem[L_END];
  LAS unsigned char* L = (LAS unsigned char*)smem;
  const int tid = threadIdx.x, lane = tid & 63, w = __builtin_amdgcn_readfirstlane(tid >> 6);
  const int c = blockIdx.x, g = c & 31, s = c >> 5, br = g >> 4, b = (g >> 2) & 3, kvh = g & 3, m = b * 8 + s;
  const int g4 = lane >> 4, l16 = lane & 15;
  const rsrc_t rK = mkr(br ? p.k1 : p.k0, KV_BYTES), rV = mkr(br ? p.v1 : p.v0, KV_BYTES);
  const rsrc_t rWQ = mkr(br ? p.wq1 : p.wq0, W_BYTES), rWO = mkr(br ? p.wo1 : p.wo0, W_BYTES);
  const rsrc_t rX = mkr(p.x, 4 * 1024 * 2), rG = mkr(p.gw, 1024 * 2), rWS = mkr(p.ws, WS_END);
  STAMP(0);

  const int kvS = (b * 4 + kvh) * (8192 * 256) + (s * 256 + w * 4) * 1024;
  const int kvV = lane * 16;
  const int wqV = (32 * ((lane >> 3) & 1) + 8 * g4) * 8192 + (lane & 7) * 16;
  const int wqS = (512 * (m & 1) + 64 * w) * 8192 + (kvh * 1024 + 64 * (m >> 1)) * 2;
  const int hO = kvh * 8 + (m >> 2), ns = m & 3, rb = w >> 1;
  const int woV = (8 * g4) * 2048 + l16 * 16;
  const int woS = (hO * 128 + 32 * rb) * 2048 + (256 * ns + 128 * (w & 1)) * 2;
  u32x4 wq[8], kd[32];
  auto issueK = [&](int j) __attribute__((always_inline)) { kd[j] = ldnt(rK, kvV, kvS + ((j >> 2) * 32 + (j & 3)) * 1024); PIN(); };

  const u32 epoch = __builtin_amdgcn_raw_buffer_load_b32(rWS, 0, WS_EPOCH, AUX_SC1);
  const int xk = 512 * (m & 1) + 64 * w + 32 * ((l16 >> 2) & 1) + 8 * g4;
  const u32x4 xf = ldpl(rX, ((lane & 3) * 1024 + xk) * 2, 0);
  const int gk = 128 * w + 32 * (l16 >> 2) + 8 * g4;
  const u32x4 xg = ldpl(rX, ((lane & 3) * 1024 + gk) * 2, 0);
  const u32x4 gf = ldpl(rG, gk * 2, 0);
  if (tid < 4) ((LAS u32*)(L + L_CNT))[tid] = 0u;
  PIN();
#pragma unroll
  for (int n = 0; n < 8; ++n) { wq[n] = ldnt(rWQ, wqV, wqS + n * 8192); PIN(); }
#pragma unroll
  for (int j = 0; j < P_D1; ++j) issueK(j);

  {
    f32x4 qa[8];
#pragma unroll
    for (int j = 0; j < 8; ++j) qa[j] = mfma(xf, colfrag(wq, j), f32x4{0.f, 0.f, 0.f, 0.f});
    STAMP(1);
    if (g4 == ((lane >> 3) & 1) && g4 < 2) {
      LAS float* red = (LAS float*)(L + L_RED) + ((w * 2 + g4) * 4) * 64 + (lane & 7) * 8;
#pragma unroll
      for (int r = 0; r < 4; ++r) {
        *(LAS f32x4*)(red + r * 64) = f32x4{qa[0][r], qa[1][r], qa[2][r], qa[3][r]};
        *(LAS f32x4*)(red + r * 64 + 4) = f32x4{qa[4][r], qa[5][r], qa[6][r], qa[7][r]};
      }
    }
    float gs = 0.f;
#pragma unroll
    for (int d = 0; d < 4; ++d) gs += rbf(bflo(xg[d]) * bflo(gf[d])) + rbf(bfhi(xg[d]) * bfhi(gf[d]));
    gs += dpp<ROR4>(gs);
    gs += dpp<ROR8>(gs);
    gs = x32(x16(gs, false), false);
    if (lane < 4) ((LAS float*)(L + L_GP))[w * 4 + lane] = gs;
  }
  const u32 tag = epoch + 1u;
  __syncthreads();
  if (w < 4) {
    const LAS float* red = (const LAS float*)(L + L_RED) + w * 64 + lane;
    float a = 0.f;
#pragma unroll
    for (int i = 0; i < 16; ++i) a += red[i * 256];
    stc2(u32x2{asu(a), tag}, rWS, lane * 8, WS_Q + ((((br * 4 + kvh) * 4 + w) * 2 + (m & 1)) * 1024 + (m >> 1) * 64) * 8);
    STAMP(2);
  }
  PIN();

  const int qS = WS_Q + (((br * 4 + kvh) * 4 + b) * 2048 + w * 128) * 8;
  u32x4 qg[2];
  auto pollq = [&]() __attribute__((always_inline)) { qg[0] = ldc4(rWS, lane * 16, qS); qg[1] = ldc4(rWS, lane * 16, qS + 8192); PIN(); };
#pragma unroll
  for (int j = P_D1; j < P_POLL; ++j) { waitvm<P_PACE - 1>(); issueK(j); }
  pollq();
#pragma unroll
  for (int j = P_POLL; j < 32; ++j) issueK(j);
  for (unsigned spin = 0; spin < kSpin; ++spin) {
    if (__all(qg[0][1] == tag && qg[0][3] == tag && qg[1][1] == tag && qg[1][3] == tag)) break;
    pollq();
  }
  STAMP(3);
  ((LAS u32*)(L + L_QS))[tid] = pkbf(asf(qg[0][0]) + asf(qg[1][0]), asf(qg[0][2]) + asf(qg[1][2]));
  __syncthreads();
  u32x4 qf[4];
#pragma unroll
  for (int kk = 0; kk < 4; ++kk) qf[kk] = *(const LAS u32x4*)(L + L_QS + (l16 & 7) * 256 + kk * 64 + g4 * 16);
  STAMP(4);

  constexpr float LOG2E = 1.4426950408889634f;
  constexpr float SC = P_MIMIC ? LOG2E : 0.08838834764831845f * LOG2E;
  const int orow = ((br * 32 + kvh * 8) * 4 + b) * 8 + s;
  u32x4 wo[8];
  auto attend = [&](auto role) __attribute__((always_inline)) {
    constexpr bool LATE = decltype(role)::value != 0;
    constexpr int NEARLY = LATE ? P_NE : 8;
    u32x4 vd[32];
    auto issueV = [&](int u) __attribute__((always_inline)) { vd[u] = ldnt(rV, kvV, kvS + ((u >> 2) * 32 + (u & 3)) * 1024); PIN(); };
    auto issueWO = [&]() __attribute__((always_inline)) {
#pragma unroll
      for (int i = 0; i < 8; ++i) { wo[i] = ldnt(rWO, woV, woS + i * 2048); PIN(); }
    };
    f32x4 S[8];
    LAS unsigned char* kst = L + L_KST + w * 8192;
#pragma unroll
    for (int T = 0; T < 8; ++T) {
      LAS unsigned char* kb = kst + (T & 1) * 4096;
#pragma unroll
      for (int i = 0; i < 4; ++i) {
        const int rho = 4 * g4 + i;
        *(LAS u32x4*)(kb + rho * 256 + ((l16 ^ rho) * 16)) = kd[4 * T + i];
      }
      if (T < NEARLY) {
        PIN();
#pragma unroll
        for (int i = 0; i < 4; ++i) issueV(4 * T + i);
      }
      __builtin_amdgcn_fence(__ATOMIC_RELEASE, "wavefront");
      __builtin_amdgcn_wave_barrier();
      __builtin_amdgcn_fence(__ATOMIC_ACQUIRE, "wavefront");
      f32x4 a = {0.f, 0.f, 0.f, 0.f};
#pragma unroll
      for (int kk = 0; kk < 4; ++kk) {
        const u32x4 kf = *(const LAS u32x4*)(kb + l16 * 256 + (((4 * kk + g4) ^ l16) * 16));
        a = mfma(kf, qf[kk], a);
      }
      const u32 r0 = pkbf(a[0], a[1]), r1 = pkbf(a[2], a[3]);
      const u32 t0 = pkbf(bflo(r0) * 0.08838834764831845f, bfhi(r0) * 0.08838834764831845f);
      const u32 t1 = pkbf(bflo(r1) * 0.08838834764831845f, bfhi(r1) * 0.08838834764831845f);
      a = f32x4{bflo(t0), bfhi(t0), bflo(t1), bfhi(t1)};
      S[T] = a;
    }
    STAMP(5);
    float mx = S[0][0];
#pragma unroll
    for (int T = 0; T < 8; ++T)
#pragma unroll
      for (int r = 0; r < 4; ++r) mx = fmaxf(mx, S[T][r]);
    mx = x32(x16(mx, true), true);
    const float mxs = mx * SC;
    float ls = 0.f;
    u32x4 pf[4];
#pragma unroll
    for (int T = 0; T < 8; ++T) {
      float e[4];
#pragma unroll
      for (int r = 0; r < 4; ++r) { e[r] = __builtin_amdgcn_exp2f(S[T][r] * SC - mxs); ls += e[r]; }
      if (!F_RESC || T < 6) {
        pf[T >> 1][(T & 1) * 2] = pkbf(e[0], e[1]);
        pf[T >> 1][(T & 1) * 2 + 1] = pkbf(e[2], e[3]);
      }
    }
    ls = x32(x16(ls, false), false);
    if (lane < 8) *(LAS f32x2*)(L + L_ML + (w * 8 + lane) * 8) = f32x2{mxs, ls};
    if (F_RESC) lds_inc(L + L_CNT + 4);
    PIN();
    if (LATE) {
#pragma unroll
      for (int u = 4 * NEARLY; u < 32; ++u) issueV(u);
    }
    STAMP(6);
    f32x4 acc[8];
#pragma unroll
    for (int j = 0; j < 8; ++j) acc[j] = f32x4{0.f, 0.f, 0.f, 0.f};
    auto consumeV = [&](int t) __attribute__((always_inline)) {
#pragma unroll
      for (int j = 0; j < 8; ++j) acc[j] = mfma(colfrag(vd + 8 * t, j), pf[t], acc[j]);
      PIN();
    };
    consumeV(0);
    issueWO();
    consumeV(1);
    consumeV(2);
    consumeV(3);
    STAMP(7);
    if (l16 < 8) {
      LAS float* pr = (LAS float*)(L + L_PART) + (w * 8 + l16) * PSTR + 32 * g4;
#pragma unroll
      for (int r = 0; r < 4; ++r) {
        *(LAS f32x4*)(pr + 8 * r) = f32x4{acc[0][r], acc[1][r], acc[2][r], acc[3][r]};
        *(LAS f32x4*)(pr + 8 * r + 4) = f32x4{acc[4][r], acc[5][r], acc[6][r], acc[7][r]};
      }
    }
  };
  attend(IC<1>{});
  __syncthreads();
  {
    f32x2 o[8];
#pragma unroll
    for (int i = 0; i < 8; ++i) o[i] = *(const LAS f32x2*)((const LAS float*)(L + L_PART) + (i * 8 + w) * PSTR + 2 * lane);
    const f32x2 ml = *(const LAS f32x2*)(L + L_ML + ((lane & 7) * 8 + w) * 8);
    float M = ml[0];
    M = fmaxf(M, dpp<XOR1>(M)); M = fmaxf(M, dpp<XOR2>(M)); M = fmaxf(M, dpp<XOR7>(M));
    const float sc = __builtin_amdgcn_exp2f(ml[0] - M);
    float Ls = sc * ml[1];
    Ls += dpp<XOR1>(Ls); Ls += dpp<XOR2>(Ls); Ls += dpp<XOR7>(Ls);
    f32x2 t = {0.f, 0.f};
#pragma unroll
    for (int i = 0; i < 8; ++i) t += asf(__builtin_amdgcn_readlane(asu(sc), i)) * o[i];
    if (lane == 0) stc4(u32x4{asu(M), tag, asu(Ls), tag}, rWS, (orow + 32 * w) * 16, WS_OS);
    stc2(u32x2{pkbf(t[0], t[1]), tag}, rWS, lane * 8, WS_OD + (orow + 32 * w) * 512);
  }
  STAMP(8);
  u32x4 wf[8];
#pragma unroll
  for (int j = 0; j < 8; ++j) { wf[j] = colfrag(wo, j); asm volatile("" : "+v"(wf[j])); }
  float gt, gn;
  {
    float z = 0.f;
#pragma unroll
    for (int i = 0; i < 8; ++i) z += ((const LAS float*)(L + L_GP))[i * 4 + (c >> 6)];
    z = rbf(z);
    gt = rbf(1.f / (1.f + __expf(-z)));
    gn = rbf(1.f - gt);
    asm volatile("" : "+v"(gt), "+v"(gn));
  }
  PIN();
  if (w < 4) {
    const int row0 = ((br * 32 + hO) * 4 + w) * 8;
    const int dV = (lane >> 5) * 512 + (lane & 31) * 16;
    u32x4 d[4], sv;
    bool oks = false, okd[4] = {false, false, false, false};
    for (unsigned spin = 0; spin < kSpin; ++spin) {
      if (!oks) { sv = ldc4(rWS, (lane & 7) * 16, WS_OS + row0 * 16); }
#pragma unroll
      for (int j = 0; j < 4; ++j) if (!okd[j]) d[j] = ldc4(rWS, dV, WS_OD + row0 * 512 + j * 1024);
      oks = sv[1] == tag && sv[3] == tag;
      bool ok = oks;
#pragma unroll
      for (int j = 0; j < 4; ++j) { okd[j] = d[j][1] == tag && d[j][3] == tag; ok = ok && okd[j]; }
      PIN();
      const int missing = __builtin_popcountll(__ballot(!ok));
      if (missing == 0) break;
      if (missing > P_TH1) __builtin_amdgcn_s_sleep(P_SLP); else if (P_SLT) __builtin_amdgcn_s_sleep(P_SLT);
    }
    STAMP(9);
    float M = asf(sv[0]);
    M = fmaxf(M, dpp<XOR1>(M)); M = fmaxf(M, dpp<XOR2>(M)); M = fmaxf(M, dpp<XOR7>(M));
    const float sc = __builtin_amdgcn_exp2f(asf(sv[0]) - M);
    float Ls = sc * asf(sv[2]);
    Ls += dpp<XOR1>(Ls); Ls += dpp<XOR2>(Ls); Ls += dpp<XOR7>(Ls);
    const float cf = sc * __builtin_amdgcn_rcpf(Ls);
    f32x4 o = {0.f, 0.f, 0.f, 0.f};
#pragma unroll
    for (int j = 0; j < 4; ++j) {
      const float c0 = asf(__builtin_amdgcn_readlane(asu(cf), 2 * j)), c1 = asf(__builtin_amdgcn_readlane(asu(cf), 2 * j + 1));
      const float cj = lane < 32 ? c0 : c1;
      o += cj * f32x4{bflo(d[j][0]), bfhi(d[j][0]), bflo(d[j][2]), bfhi(d[j][2])};
    }
#pragma unroll
    for (int e = 0; e < 4; ++e) o[e] = x32(o[e], false);
    if (lane < 32) *(LAS u32x2*)(L + L_OB + w * 256 + lane * 8) = u32x2{pkbf(o[0], o[1]), pkbf(o[2], o[3])};
  }
  __syncthreads();
  {
    const u32x4 of = *(const LAS u32x4*)(L + L_OB + (l16 & 3) * 256 + (32 * rb + 8 * g4) * 2);
    f32x4 oa[8];
#pragma unroll
    for (int j = 0; j < 8; ++j) oa[j] = mfma(wf[j], of, f32x4{0.f, 0.f, 0.f, 0.f});
    if (l16 < 4) {
      LAS float* red = (LAS float*)(L + L_RED) + (rb * 4 + l16) * 260 + 128 * (w & 1) + 32 * g4;
#pragma unroll
      for (int r = 0; r < 4; ++r) {
        *(LAS f32x4*)(red + 8 * r) = f32x4{oa[0][r], oa[1][r], oa[2][r], oa[3][r]};
        *(LAS f32x4*)(red + 8 * r + 4) = f32x4{oa[4][r], oa[5][r], oa[6][r], oa[7][r]};
      }
    }
  }
  __syncthreads();
  {
    const int bb = tid >> 7, cp = tid & 127;
    const LAS float* red = (const LAS float*)(L + L_RED) + bb * 260 + 2 * cp;
    const f32x2 o = (*(const LAS f32x2*)(red) + *(const LAS f32x2*)(red + 1040)) + (*(const LAS f32x2*)(red + 2080) + *(const LAS f32x2*)(red + 3120));
    const int r = (bb * 4 + ns) * 16 + (cp >> 3);
    stc4(u32x4{asu(o[0]), tag, asu(o[1]), tag}, rWS, ((r * 64) * 16 + 2 * (cp & 7)) * 8, WS_F + ((br * 32 + hO) * 16) * 8);
  }
  STAMP(10);

  {
    u32x4 d;
    bool ok = false;
    for (unsigned spin = 0; spin < kSpin; ++spin) {
      if (!ok) {
        d = ldc4(rWS, lane * 16, WS_F + c * 8192 + w * 1024);
        ok = d[1] == tag && d[3] == tag;
      }
      PIN();
      const int missing = __builtin_popcountll(__ballot(!ok));
      if (missing == 0) break;
      if (missing > P_TH2) __builtin_amdgcn_s_sleep(P_SLP2); else if (P_SLT) __builtin_amdgcn_s_sleep(P_SLT);
    }
    STAMP(11);
    float a0 = asf(d[0]), a1 = asf(d[2]);
    a0 = x32(x16(a0 + dpp<ROR8>(a0), false), false);
    a1 = x32(x16(a1 + dpp<ROR8>(a1), false), false);
    if (lane < 8) *(LAS f32x2*)(L + L_FIN + w * 64 + lane * 8) = f32x2{a0, a1};
    if (lds_inc(L + L_CNT + 8) == 7u) {
      const LAS f32x2* fin = (const LAS f32x2*)(L + L_FIN) + (lane & 7);
      const f32x2 a = (fin[0] + fin[8]) + (fin[16] + fin[24]);
      const f32x2 bq = (fin[32] + fin[40]) + (fin[48] + fin[56]);
      const int bb = c >> 6;
      const float o0 = rbf(gt * rbf(a[0])) + rbf(gn * rbf(bq[0]));
      const float o1 = rbf(gt * rbf(a[1])) + rbf(gn * rbf(bq[1]));
      if (lane < 8) ((u32*)p.out)[bb * 512 + ((c >> 4) & 3) * 128 + (c & 15) * 8 + lane] = pkbf(o0, o1);
      if (c == 0 && lane == 0) __builtin_amdgcn_raw_buffer_store_b32(tag, rWS, 0, WS_EPOCH, AUX_SC1);
      STAMP(12);
    }
  }
  STAMP_FLUSH();
}

extern "C" void kernel_launch(void* const* d_in, const int* in_sizes, int n_in, void* d_out, int out_size, void* d_ws, size_t ws_size,
                              hipStream_t stream) {
  Params p;
  p.x = d_in[0]; p.gw = d_in[10];
  p.k0 = d_in[1]; p.v0 = d_in[2]; p.wq0 = d_in[3]; p.wo0 = d_in[4];
  p.k1 = d_in[6]; p.v1 = d_in[7]; p.wq1 = d_in[8]; p.wo1 = d_in[9];
  p.out = d_out; p.ws = d_ws;
  hipLaunchKernelGGL(gated_mix_decode_kernel, dim3(256), dim3(512), 0, stream, p);
}
```

```cpp
#include <hip/hip_runtime.h>
#include <hip/hip_bf16.h>
#include <stdint.h>

typedef unsigned short u16;
typedef unsigned int u32;
typedef unsigned long long u64;
typedef u32 u32x2 __attribute__((ext_vector_type(2)));
typedef u32 u32x4 __attribute__((ext_vector_type(4)));
typedef float f32x2 __attribute__((ext_vector_type(2)));
typedef float f32x4 __attribute__((ext_vector_type(4)));
typedef short bf16x8 __attribute__((ext_vector_type(8)));
typedef __amdgpu_buffer_rsrc_t rsrc_t;
#define LAS __attribute__((address_space(3)))

#define P_D1 4
#define P_PACE 6
#define P_POLL 16
#define F_AG 0
#define F_ROLE 2
#define F_RESC 0
#define P_NE 6
#define F_PP 0
#define P_PPS 3
#define P_TH1 32
#define P_TH2 16
#define P_SLP2 P_SLP
#define P_SLT 0
#define F_SKEW 1
#define F_SKEWX 0
#define P_LSL 2
#define P_SLP 4
#define P_MIMIC 1

constexpr int KV_BYTES = 4 * 4 * 8192 * 128 * 2;
constexpr int W_BYTES = 1024 * 4096 * 2;
constexpr int AUX_NT = 2, AUX_SC1 = 16;

constexpr int WS_EPOCH = 0;
constexpr int WS_Q = 4096;
constexpr int WS_OD = WS_Q + 2 * 4 * 4 * 2048 * 8;
constexpr int WS_OS = WS_OD + 2 * 32 * 4 * 8 * 64 * 8;
constexpr int WS_F = WS_OS + 2 * 32 * 4 * 8 * 16;
constexpr int WS_END = WS_F + 256 * 64 * 16 * 8;
constexpr int WS_STAMP = 8 << 20;

constexpr int L_KST = 0;
constexpr int L_RED = L_KST + 8 * 8192;
constexpr int L_QS = L_RED + 16640;
constexpr int PSTR = 132;
constexpr int L_PART = L_QS + 2048;
constexpr int L_ML = L_PART + 8 * 8 * PSTR * 4;
constexpr int L_OB = L_ML + 512;
constexpr int L_FIN = L_OB + 1024;
constexpr int L_GP = L_FIN + 512;
constexpr int L_CNT = L_GP + 128;
constexpr int L_END = L_CNT + 16;

struct Params {
  const void* x; const void* gw;
  const void* k0; const void* v0; const void* wq0; const void* wo0;
  const void* k1; const void* v1; const void* wq1; const void* wo1;
  void* out; void* ws;
};

__device__ __forceinline__ rsrc_t mkr(const void* p, int bytes) { return __builtin_amdgcn_make_buffer_rsrc((void*)p, 0, bytes, 0x00020000); }
__device__ __forceinline__ u32x4 ldnt(rsrc_t r, int voff, int soff) { return __builtin_amdgcn_raw_buffer_load_b128(r, voff, soff, AUX_NT); }
__device__ __forceinline__ u32x4 ldpl(rsrc_t r, int voff, int soff) { return __builtin_amdgcn_raw_buffer_load_b128(r, voff, soff, 0); }
__device__ __forceinline__ u32x4 ldc4(rsrc_t r, int voff, int soff) { return __builtin_amdgcn_raw_buffer_load_b128(r, voff, soff, AUX_SC1); }
__device__ __forceinline__ void stc4(u32x4 v, rsrc_t r, int voff, int soff) {
  __builtin_amdgcn_raw_buffer_store_b128(v, r, voff + soff, 0, AUX_SC1);
  __builtin_amdgcn_sched_barrier(0);
  asm volatile("s_nop 1");
  __builtin_amdgcn_sched_barrier(0);
}
__device__ __forceinline__ void stc2(u32x2 v, rsrc_t r, int voff, int soff) { __builtin_amdgcn_raw_buffer_store_b64(v, r, voff, soff, AUX_SC1); }
__device__ __forceinline__ f32x4 mfma(u32x4 a, u32x4 b, f32x4 c) {
  return __builtin_amdgcn_mfma_f32_16x16x32_bf16(__builtin_bit_cast(bf16x8, a), __builtin_bit_cast(bf16x8, b), c, 0, 0, 0);
}
__device__ __forceinline__ float asf(u32 u) { return __builtin_bit_cast(float, u); }
__device__ __forceinline__ u32 asu(float f) { return __builtin_bit_cast(u32, f); }
typedef __bf16 bf2_t __attribute__((ext_vector_type(2)));
__device__ __forceinline__ u32 pkbf(float lo, float hi) {
  const f32x2 v = {lo, hi};
  return __builtin_bit_cast(u32, __builtin_convertvector(v, bf2_t));
}
__device__ __forceinline__ float bflo(u32 u) { return asf(u << 16); }
__device__ __forceinline__ float bfhi(u32 u) { return asf(u & 0xffff0000u); }
__device__ __forceinline__ float rbf(float f) { return bflo(pkbf(f, 0.f)); }
__device__ __forceinline__ float x16(float v, bool mx) {
  auto s = __builtin_amdgcn_permlane16_swap(asu(v), asu(v), false, false);
  return mx ? fmaxf(asf(s[0]), asf(s[1])) : asf(s[0]) + asf(s[1]);
}
__device__ __forceinline__ float x32(float v, bool mx) {
  auto s = __builtin_amdgcn_permlane32_swap(asu(v), asu(v), false, false);
  return mx ? fmaxf(asf(s[0]), asf(s[1])) : asf(s[0]) + asf(s[1]);
}
template <int CTRL> __device__ __forceinline__ float dpp(float v) {
  return asf(__builtin_amdgcn_mov_dpp(asu(v), CTRL, 0xf, 0xf, true));
}
constexpr int ROR4 = 0x124, ROR8 = 0x128, XOR1 = 0xB1, XOR2 = 0x4E, XOR7 = 0x141;
template <int N> struct IC { static constexpr int value = N; };
#define SB() __builtin_amdgcn_sched_barrier(0)
#define PIN() do { asm volatile("" ::: "memory"); __builtin_amdgcn_sched_barrier(0); } while (0)
template <int N> __device__ __forceinline__ void waitvm() { asm volatile("s_waitcnt vmcnt(%0)" ::"n"(N) : "memory"); }
constexpr unsigned kSpin = 1u << 18;
__device__ __forceinline__ u32 lds_inc(LAS unsigned char* p) {
  u32 old = 0;
  if ((threadIdx.x & 63) == 0) old = __hip_atomic_fetch_add((LAS u32*)p, 1u, __ATOMIC_ACQ_REL, __HIP_MEMORY_SCOPE_WORKGROUP);
  return __builtin_amdgcn_readfirstlane(old);
}

__device__ __forceinline__ u32x4 colfrag(const u32x4* v, int j) {
  const u32 sel = (j & 1) ? 0x07060302u : 0x05040100u;
  const int d = j >> 1;
  u32x4 f;
  f[0] = __builtin_amdgcn_perm(v[1][d], v[0][d], sel);
  f[1] = __builtin_amdgcn_perm(v[3][d], v[2][d], sel);
  f[2] = __builtin_amdgcn_perm(v[5][d], v[4][d], sel);
  f[3] = __builtin_amdgcn_perm(v[7][d], v[6][d], sel);
  return f;
}

#define STAMP(i) do { } while (0)
#define STAMP_FLUSH() do { } while (0)

__global__ void __launch_bounds__(512, 2) gated_mix_decode_kernel(Params p) {
  __shared__ __attribute__((aligned(16))) unsigned char smem[L_END];
  LAS unsigned char* L = (LAS unsigned char*)smem;
  const int tid = threadIdx.x, lane = tid & 63, w = __builtin_amdgcn_readfirstlane(tid >> 6);
  const int c = blockIdx.x, g = c & 31, s = c >> 5, br = g >> 4, b = (g >> 2) & 3, kvh = g & 3, m = b * 8 + s;
  const int g4 = lane >> 4, l16 = lane & 15;
  const rsrc_t rK = mkr(br ? p.k1 : p.k0, KV_BYTES), rV = mkr(br ? p.v1 : p.v0, KV_BYTES);
  const rsrc_t rWQ = mkr(br ? p.wq1 : p.wq0, W_BYTES), rWO = mkr(br ? p.wo1 : p.wo0, W_BYTES);
  const rsrc_t rX = mkr(p.x, 4 * 1024 * 2), rG = mkr(p.gw, 1024 * 2), rWS = mkr(p.ws, WS_END);
  STAMP(0);

  const int kvS = (b * 4 + kvh) * (8192 * 256) + (s * 256 + w * 4) * 1024;
  const int kvV = lane * 16;
  const int kh = (c ^ F_SKEWX) & 1, cbk = (c >> 1) & 15, kvhq = (c >> 5) & 3, brq = c >> 7;
  const int sl0 = kh ? 12 + (w & 3) : w, sl1 = 8 + (w & 3);
  const bool has0 = !kh || w < 4, has1 = !kh && w < 4;
  const int wqL = (32 * ((lane >> 3) & 1) + 8 * g4) * 8192 + (lane & 7) * 16;
  const int wqV0 = has0 ? wqL : 0x40000000, wqV1 = has1 ? wqL : 0x40000000;
  const int wqS0 = (64 * sl0) * 8192 + (kvhq * 1024 + 64 * cbk) * 2, wqS1 = (64 * sl1) * 8192 + (kvhq * 1024 + 64 * cbk) * 2;
  const rsrc_t rWQ2 = mkr(brq ? p.wq1 : p.wq0, W_BYTES);
  const int hO = kvh * 8 + (m >> 2), ns = m & 3, rb = w >> 1;
  const int woV = (8 * g4) * 2048 + l16 * 16;
  const int woS = (hO * 128 + 32 * rb) * 2048 + (256 * ns + 128 * (w & 1)) * 2;
  u32x4 wq[8], kd[32];
  auto issueK = [&](int j) __attribute__((always_inline)) { kd[j] = ldnt(rK, kvV, kvS + ((j >> 2) * 32 + (j & 3)) * 1024); PIN(); };

  const u32 epoch = __builtin_amdgcn_raw_buffer_load_b32(rWS, 0, WS_EPOCH, AUX_SC1);
  const int xo = 32 * ((l16 >> 2) & 1) + 8 * g4;
  const u32x4 xf = ldpl(rX, ((lane & 3) * 1024 + 64 * sl0 + xo) * 2, 0);
  const u32x4 xf1 = ldpl(rX, ((lane & 3) * 1024 + 64 * sl1 + xo) * 2, 0);
  const int gk = 128 * w + 32 * (l16 >> 2) + 8 * g4;
  const u32x4 xg = ldpl(rX, ((lane & 3) * 1024 + gk) * 2, 0);
  const u32x4 gf = ldpl(rG, gk * 2, 0);
  if (tid < 4) ((LAS u32*)(L + L_CNT))[tid] = 0u;
  PIN();
  u32x4 wq1[8];
#pragma unroll
  for (int n = 0; n < 8; ++n) { wq[n] = ldnt(rWQ2, wqV0, wqS0 + n * 8192); PIN(); }
#pragma unroll
  for (int n = 0; n < 8; ++n) { wq1[n] = ldnt(rWQ2, wqV1, wqS1 + n * 8192); PIN(); }
#pragma unroll
  for (int j = 0; j < P_D1; ++j) issueK(j);

  {
    f32x4 qa[8];
#pragma unroll
    for (int j = 0; j < 8; ++j) qa[j] = mfma(xf, colfrag(wq, j), f32x4{0.f, 0.f, 0.f, 0.f});
#pragma unroll
    for (int j = 0; j < 8; ++j) qa[j] = mfma(xf1, colfrag(wq1, j), qa[j]);
    STAMP(1);
    if (g4 == ((lane >> 3) & 1) && g4 < 2) {
      LAS float* red = (LAS float*)(L + L_RED) + ((w * 2 + g4) * 4) * 64 + (lane & 7) * 8;
#pragma unroll
      for (int r = 0; r < 4; ++r) {
        *(LAS f32x4*)(red + r * 64) = f32x4{qa[0][r], qa[1][r], qa[2][r], qa[3][r]};
        *(LAS f32x4*)(red + r * 64 + 4) = f32x4{qa[4][r], qa[5][r], qa[6][r], qa[7][r]};
      }
    }
    float gs = 0.f;
#pragma unroll
    for (int d = 0; d < 4; ++d) gs += rbf(bflo(xg[d]) * bflo(gf[d])) + rbf(bfhi(xg[d]) * bfhi(gf[d]));
    gs += dpp<ROR4>(gs);
    gs += dpp<ROR8>(gs);
    gs = x32(x16(gs, false), false);
    if (lane < 4) ((LAS float*)(L + L_GP))[w * 4 + lane] = gs;
  }
  const u32 tag = epoch + 1u;
  __syncthreads();
  if (w < 4) {
    const LAS float* red = (const LAS float*)(L + L_RED) + w * 64 + lane;
    float a = 0.f;
#pragma unroll
    for (int i = 0; i < 16; ++i) a += red[i * 256];
    stc2(u32x2{asu(a), tag}, rWS, lane * 8, WS_Q + ((((brq * 4 + kvhq) * 4 + w) * 2 + kh) * 1024 + cbk * 64) * 8);
    STAMP(2);
  }
  PIN();

  const int qS = WS_Q + (((br * 4 + kvh) * 4 + b) * 2048 + w * 128) * 8;
  u32x4 qg[2];
  auto pollq = [&]() __attribute__((always_inline)) { qg[0] = ldc4(rWS, lane * 16, qS); qg[1] = ldc4(rWS, lane * 16, qS + 8192); PIN(); };
#pragma unroll
  for (int j = P_D1; j < P_POLL; ++j) { waitvm<P_PACE - 1>(); issueK(j); }
  pollq();
#pragma unroll
  for (int j = P_POLL; j < 32; ++j) issueK(j);
  for (unsigned spin = 0; spin < kSpin; ++spin) {
    if (__all(qg[0][1] == tag && qg[0][3] == tag && qg[1][1] == tag && qg[1][3] == tag)) break;
    pollq();
  }
  STAMP(3);
  ((LAS u32*)(L + L_QS))[tid] = pkbf(asf(qg[0][0]) + asf(qg[1][0]), asf(qg[0][2]) + asf(qg[1][2]));
  __syncthreads();
  u32x4 qf[4];
#pragma unroll
  for (int kk = 0; kk < 4; ++kk) qf[kk] = *(const LAS u32x4*)(L + L_QS + (l16 & 7) * 256 + kk * 64 + g4 * 16);
  STAMP(4);

  constexpr float LOG2E = 1.4426950408889634f;
  constexpr float SC = P_MIMIC ? LOG2E : 0.08838834764831845f * LOG2E;
  const int orow = ((br * 32 + kvh * 8) * 4 + b) * 8 + s;
  u32x4 wo[8];
  auto attend = [&](auto role) __attribute__((always_inline)) {
    constexpr bool LATE = decltype(role)::value != 0;
    constexpr int NEARLY = LATE ? P_NE : 8;
    u32x4 vd[32];
    auto issueV = [&](int u) __attribute__((always_inline)) { vd[u] = ldnt(rV, kvV, kvS + ((u >> 2) * 32 + (u & 3)) * 1024); PIN(); };
    auto issueWO = [&]() __attribute__((always_inline)) {
#pragma unroll
      for (int i = 0; i < 8; ++i) { wo[i] = ldnt(rWO, woV, woS + i * 2048); PIN(); }
    };
    f32x4 S[8];
    LAS unsigned char* kst = L + L_KST + w * 8192;
#pragma unroll
    for (int T = 0; T < 8; ++T) {
      LAS unsigned char* kb = kst + (T & 1) * 4096;
#pragma unroll
      for (int i = 0; i < 4; ++i) {
        const int rho = 4 * g4 + i;
        *(LAS u32x4*)(kb + rho * 256 + ((l16 ^ rho) * 16)) = kd[4 * T + i];
      }
      if (T < NEARLY) {
        PIN();
#pragma unroll
        for (int i = 0; i < 4; ++i) issueV(4 * T + i);
      }
      __builtin_amdgcn_fence(__ATOMIC_RELEASE, "wavefront");
      __builtin_amdgcn_wave_barrier();
      __builtin_amdgcn_fence(__ATOMIC_ACQUIRE, "wavefront");
      f32x4 a = {0.f, 0.f, 0.f, 0.f};
#pragma unroll
      for (int kk = 0; kk < 4; ++kk) {
        const u32x4 kf = *(const LAS u32x4*)(kb + l16 * 256 + (((4 * kk + g4) ^ l16) * 16));
        a = mfma(kf, qf[kk], a);
      }
      const u32 r0 = pkbf(a[0], a[1]), r1 = pkbf(a[2], a[3]);
      const u32 t0 = pkbf(bflo(r0) * 0.08838834764831845f, bfhi(r0) * 0.08838834764831845f);
      const u32 t1 = pkbf(bflo(r1) * 0.08838834764831845f, bfhi(r1) * 0.08838834764831845f);
      a = f32x4{bflo(t0), bfhi(t0), bflo(t1), bfhi(t1)};
      S[T] = a;
    }
    STAMP(5);
    float mx = S[0][0];
#pragma unroll
    for (int T = 0; T < 8; ++T)
#pragma unroll
      for (int r = 0; r < 4; ++r) mx = fmaxf(mx, S[T][r]);
    mx = x32(x16(mx, true), true);
    const float mxs = mx * SC;
    float ls = 0.f;
    u32x4 pf[4];
#pragma unroll
    for (int T = 0; T < 8; ++T) {
      float e[4];
#pragma unroll
      for (int r = 0; r < 4; ++r) { e[r] = __builtin_amdgcn_exp2f(S[T][r] * SC - mxs); ls += e[r]; }
      if (!F_RESC || T < 6) {
        pf[T >> 1][(T & 1) * 2] = pkbf(e[0], e[1]);
        pf[T >> 1][(T & 1) * 2 + 1] = pkbf(e[2], e[3]);
      }
    }
    ls = x32(x16(ls, false), false);
    if (lane < 8) *(LAS f32x2*)(L + L_ML + (w * 8 + lane) * 8) = f32x2{mxs, ls};
    if (F_RESC) lds_inc(L + L_CNT + 4);
    PIN();
    if (LATE) {
#pragma unroll
      for (int u = 4 * NEARLY; u < 32; ++u) issueV(u);
    }
    STAMP(6);
    f32x4 acc[8];
#pragma unroll
    for (int j = 0; j < 8; ++j) acc[j] = f32x4{0.f, 0.f, 0.f, 0.f};
    auto consumeV = [&](int t) __attribute__((always_inline)) {
#pragma unroll
      for (int j = 0; j < 8; ++j) acc[j] = mfma(colfrag(vd + 8 * t, j), pf[t], acc[j]);
      PIN();
    };
    consumeV(0);
    issueWO();
    consumeV(1);
    consumeV(2);
    consumeV(3);
    STAMP(7);
    if (l16 < 8) {
      LAS float* pr = (LAS float*)(L + L_PART) + (w * 8 + l16) * PSTR + 32 * g4;
#pragma unroll
      for (int r = 0; r < 4; ++r) {
        *(LAS f32x4*)(pr + 8 * r) = f32x4{acc[0][r], acc[1][r], acc[2][r], acc[3][r]};
        *(LAS f32x4*)(pr + 8 * r + 4) = f32x4{acc[4][r], acc[5][r], acc[6][r], acc[7][r]};
      }
    }
  };
  attend(IC<1>{});
  __syncthreads();
  {
    f32x2 o[8];
#pragma unroll
    for (int i = 0; i < 8; ++i) o[i] = *(const LAS f32x2*)((const LAS float*)(L + L_PART) + (i * 8 + w) * PSTR + 2 * lane);
    const f32x2 ml = *(const LAS f32x2*)(L + L_ML + ((lane & 7) * 8 + w) * 8);
    float M = ml[0];
    M = fmaxf(M, dpp<XOR1>(M)); M = fmaxf(M, dpp<XOR2>(M)); M = fmaxf(M, dpp<XOR7>(M));
    const float sc = __builtin_amdgcn_exp2f(ml[0] - M);
    float Ls = sc * ml[1];
    Ls += dpp<XOR1>(Ls); Ls += dpp<XOR2>(Ls); Ls += dpp<XOR7>(Ls);
    f32x2 t = {0.f, 0.f};
#pragma unroll
    for (int i = 0; i < 8; ++i) t += asf(__builtin_amdgcn_readlane(asu(sc), i)) * o[i];
    if (lane == 0) stc4(u32x4{asu(M), tag, asu(Ls), tag}, rWS, (orow + 32 * w) * 16, WS_OS);
    stc2(u32x2{pkbf(t[0], t[1]), tag}, rWS, lane * 8, WS_OD + (orow + 32 * w) * 512);
  }
  STAMP(8);
  u32x4 wf[8];
#pragma unroll
  for (int j = 0; j < 8; ++j) { wf[j] = colfrag(wo, j); asm volatile("" : "+v"(wf[j])); }
  float gt, gn;
  {
    float z = 0.f;
#pragma unroll
    for (int i = 0; i < 8; ++i) z += ((const LAS float*)(L + L_GP))[i * 4 + (c >> 6)];
    z = rbf(z);
    gt = rbf(1.f / (1.f + __expf(-z)));
    gn = rbf(1.f - gt);
    asm volatile("" : "+v"(gt), "+v"(gn));
  }
  PIN();
  if (w < 4) {
    const int row0 = ((br * 32 + hO) * 4 + w) * 8;
    const int dV = (lane >> 5) * 512 + (lane & 31) * 16;
    u32x4 d[4], sv;
    bool oks = false, okd[4] = {false, false, false, false};
    for (unsigned spin = 0; spin < kSpin; ++spin) {
      if (!oks) { sv = ldc4(rWS, (lane & 7) * 16, WS_OS + row0 * 16); }
#pragma unroll
      for (int j = 0; j < 4; ++j) if (!okd[j]) d[j] = ldc4(rWS, dV, WS_OD + row0 * 512 + j * 1024);
      oks = sv[1] == tag && sv[3] == tag;
      bool ok = oks;
#pragma unroll
      for (int j = 0; j < 4; ++j) { okd[j] = d[j][1] == tag && d[j][3] == tag; ok = ok && okd[j]; }
      PIN();
      const int missing = __builtin_popcountll(__ballot(!ok));
      if (missing == 0) break;
      if (missing > P_TH1) __builtin_amdgcn_s_sleep(P_SLP); else if (P_SLT) __builtin_amdgcn_s_sleep(P_SLT);
    }
    STAMP(9);
    float M = asf(sv[0]);
    M = fmaxf(M, dpp<XOR1>(M)); M = fmaxf(M, dpp<XOR2>(M)); M = fmaxf(M, dpp<XOR7>(M));
    const float sc = __builtin_amdgcn_exp2f(asf(sv[0]) - M);
    float Ls = sc * asf(sv[2]);
    Ls += dpp<XOR1>(Ls); Ls += dpp<XOR2>(Ls); Ls += dpp<XOR7>(Ls);
    const float cf = sc * __builtin_amdgcn_rcpf(Ls);
    f32x4 o = {0.f, 0.f, 0.f, 0.f};
#pragma unroll
    for (int j = 0; j < 4; ++j) {
      const float c0 = asf(__builtin_amdgcn_readlane(asu(cf), 2 * j)), c1 = asf(__builtin_amdgcn_readlane(asu(cf), 2 * j + 1));
      const float cj = lane < 32 ? c0 : c1;
      o += cj * f32x4{bflo(d[j][0]), bfhi(d[j][0]), bflo(d[j][2]), bfhi(d[j][2])};
    }
#pragma unroll
    for (int e = 0; e < 4; ++e) o[e] = x32(o[e], false);
    if (lane < 32) *(LAS u32x2*)(L + L_OB + w * 256 + lane * 8) = u32x2{pkbf(o[0], o[1]), pkbf(o[2], o[3])};
  }
  __syncthreads();
  {
    const u32x4 of = *(const LAS u32x4*)(L + L_OB + (l16 & 3) * 256 + (32 * rb + 8 * g4) * 2);
    f32x4 oa[8];
#pragma unroll
    for (int j = 0; j < 8; ++j) oa[j] = mfma(wf[j], of, f32x4{0.f, 0.f, 0.f, 0.f});
    if (l16 < 4) {
      LAS float* red = (LAS float*)(L + L_RED) + (rb * 4 + l16) * 260 + 128 * (w & 1) + 32 * g4;
#pragma unroll
      for (int r = 0; r < 4; ++r) {
        *(LAS f32x4*)(red + 8 * r) = f32x4{oa[0][r], oa[1][r], oa[2][r], oa[3][r]};
        *(LAS f32x4*)(red + 8 * r + 4) = f32x4{oa[4][r], oa[5][r], oa[6][r], oa[7][r]};
      }
    }
  }
  __syncthreads();
  {
    const int bb = tid >> 7, cp = tid & 127;
    const LAS float* red = (const LAS float*)(L + L_RED) + bb * 260 + 2 * cp;
    const f32x2 o = (*(const LAS f32x2*)(red) + *(const LAS f32x2*)(red + 1040)) + (*(const LAS f32x2*)(red + 2080) + *(const LAS f32x2*)(red + 3120));
    const int r = (bb * 4 + ns) * 16 + (cp >> 3);
    stc4(u32x4{asu(o[0]), tag, asu(o[1]), tag}, rWS, ((r * 64) * 16 + 2 * (cp & 7)) * 8, WS_F + ((br * 32 + hO) * 16) * 8);
  }
  STAMP(10);

  {
    u32x4 d;
    bool ok = false;
    for (unsigned spin = 0; spin < kSpin; ++spin) {
      if (!ok) {
        d = ldc4(rWS, lane * 16, WS_F + c * 8192 + w * 1024);
        ok = d[1] == tag && d[3] == tag;
      }
      PIN();
      const int missing = __builtin_popcountll(__ballot(!ok));
      if (missing == 0) break;
      if (missing > P_TH2) __builtin_amdgcn_s_sleep(P_SLP2); else if (P_SLT) __builtin_amdgcn_s_sleep(P_SLT);
    }
    STAMP(11);
    float a0 = asf(d[0]), a1 = asf(d[2]);
    a0 = x32(x16(a0 + dpp<ROR8>(a0), false), false);
    a1 = x32(x16(a1 + dpp<ROR8>(a1), false), false);
    if (lane < 8) *(LAS f32x2*)(L + L_FIN + w * 64 + lane * 8) = f32x2{a0, a1};
    if (lds_inc(L + L_CNT + 8) == 7u) {
      const LAS f32x2* fin = (const LAS f32x2*)(L + L_FIN) + (lane & 7);
      const f32x2 a = (fin[0] + fin[8]) + (fin[16] + fin[24]);
      const f32x2 bq = (fin[32] + fin[40]) + (fin[48] + fin[56]);
      const int bb = c >> 6;
      const float o0 = rbf(gt * rbf(a[0])) + rbf(gn * rbf(bq[0]));
      const float o1 = rbf(gt * rbf(a[1])) + rbf(gn * rbf(bq[1]));
      if (lane < 8) __builtin_amdgcn_raw_buffer_store_b32(pkbf(o0, o1), mkr(p.out, 8192), (bb * 512 + ((c >> 4) & 3) * 128 + (c & 15) * 8 + lane) * 4, 0, 16);
      if (c == 0 && lane == 0) __builtin_amdgcn_raw_buffer_store_b32(tag, rWS, 0, WS_EPOCH, AUX_SC1);
      STAMP(12);
    }
  }
  STAMP_FLUSH();
}

extern "C" void kernel_launch(void* const* d_in, const int* in_sizes, int n_in, void* d_out, int out_size, void* d_ws, size_t ws_size,
                              hipStream_t stream) {
  Params p;
  p.x = d_in[0]; p.gw = d_in[10];
  p.k0 = d_in[1]; p.v0 = d_in[2]; p.wq0 = d_in[3]; p.wo0 = d_in[4];
  p.k1 = d_in[6]; p.v1 = d_in[7]; p.wq1 = d_in[8]; p.wo1 = d_in[9];
  p.out = d_out; p.ws = d_ws;
  hipLaunchKernelGGL(gated_mix_decode_kernel, dim3(256), dim3(512), 0, stream, p);
}
```

```cpp
#include <hip/hip_runtime.h>
#include <hip/hip_bf16.h>
#include <stdint.h>

typedef unsigned int u32;
typedef u32 u32x2 __attribute__((ext_vector_type(2)));
typedef u32 u32x4 __attribute__((ext_vector_type(4)));
typedef float f32x2 __attribute__((ext_vector_type(2)));
typedef float f32x4 __attribute__((ext_vector_type(4)));
typedef short bf16x8 __attribute__((ext_vector_type(8)));
typedef __bf16 bf2_t __attribute__((ext_vector_type(2)));
typedef __amdgpu_buffer_rsrc_t rsrc_t;
#define LAS __attribute__((address_space(3)))

constexpr int KV_BYTES = 4 * 4 * 8192 * 128 * 2;
constexpr int W_BYTES = 1024 * 4096 * 2;
constexpr int AUX_NT = 2, AUX_SC1 = 16;
constexpr int OOB = 0x40000000;
constexpr int K_EARLY = 4;
constexpr int K_PACE = 6;
constexpr int K_POLL = 16;
constexpr int V_EARLY = 6;
constexpr unsigned kSpin = 1u << 18;

constexpr int WS_EPOCH = 0;
constexpr int WS_Q = 4096;
constexpr int WS_OD = WS_Q + 2 * 4 * 4 * 2048 * 8;
constexpr int WS_OS = WS_OD + 2 * 32 * 4 * 8 * 64 * 8;
constexpr int WS_F = WS_OS + 2 * 32 * 4 * 8 * 16;
constexpr int WS_END = WS_F + 256 * 64 * 16 * 8;

constexpr int L_KST = 0;
constexpr int L_RED = L_KST + 8 * 8192;
constexpr int L_QS = L_RED + 16384;
constexpr int PSTR = 132;
constexpr int L_PART = L_QS + 2048;
constexpr int L_ML = L_PART + 8 * 8 * PSTR * 4;
constexpr int L_OB = L_ML + 512;
constexpr int L_FIN = L_OB + 1024;
constexpr int L_GP = L_FIN + 512;
constexpr int L_CNT = L_GP + 128;
constexpr int L_END = L_CNT + 16;

struct Params {
  const void* x; const void* gw;
  const void* k0; const void* v0; const void* wq0; const void* wo0;
  const void* k1; const void* v1; const void* wq1; const void* wo1;
  void* out; void* ws;
};

__device__ __forceinline__ rsrc_t mkr(const void* p, int bytes) { return __builtin_amdgcn_make_buffer_rsrc((void*)p, 0, bytes, 0x00020000); }
__device__ __forceinline__ u32x4 ldnt(rsrc_t r, int voff, int soff) { return __builtin_amdgcn_raw_buffer_load_b128(r, voff, soff, AUX_NT); }
__device__ __forceinline__ u32x4 ldpl(rsrc_t r, int voff) { return __builtin_amdgcn_raw_buffer_load_b128(r, voff, 0, 0); }
__device__ __forceinline__ u32x4 ldc4(rsrc_t r, int voff, int soff) { return __builtin_amdgcn_raw_buffer_load_b128(r, voff, soff, AUX_SC1); }
__device__ __forceinline__ void stc4(u32x4 v, rsrc_t r, int off) {
  __builtin_amdgcn_raw_buffer_store_b128(v, r, off, 0, AUX_SC1);
  __builtin_amdgcn_sched_barrier(0);
  asm volatile("s_nop 1");
  __builtin_amdgcn_sched_barrier(0);
}
__device__ __forceinline__ void stc2(u32x2 v, rsrc_t r, int voff, int soff) { __builtin_amdgcn_raw_buffer_store_b64(v, r, voff, soff, AUX_SC1); }
__device__ __forceinline__ f32x4 mfma(u32x4 a, u32x4 b, f32x4 c) {
  return __builtin_amdgcn_mfma_f32_16x16x32_bf16(__builtin_bit_cast(bf16x8, a), __builtin_bit_cast(bf16x8, b), c, 0, 0, 0);
}
__device__ __forceinline__ float asf(u32 u) { return __builtin_bit_cast(float, u); }
__device__ __forceinline__ u32 asu(float f) { return __builtin_bit_cast(u32, f); }
__device__ __forceinline__ u32 pkbf(float lo, float hi) {
  const f32x2 v = {lo, hi};
  return __builtin_bit_cast(u32, __builtin_convertvector(v, bf2_t));
}
__device__ __forceinline__ float bflo(u32 u) { return asf(u << 16); }
__device__ __forceinline__ float bfhi(u32 u) { return asf(u & 0xffff0000u); }
__device__ __forceinline__ float rbf(float f) { return bflo(pkbf(f, 0.f)); }
__device__ __forceinline__ float x16(float v, bool mx) {
  auto s = __builtin_amdgcn_permlane16_swap(asu(v), asu(v), false, false);
  return mx ? fmaxf(asf(s[0]), asf(s[1])) : asf(s[0]) + asf(s[1]);
}
__device__ __forceinline__ float x32(float v, bool mx) {
  auto s = __builtin_amdgcn_permlane32_swap(asu(v), asu(v), false, false);
  return mx ? fmaxf(asf(s[0]), asf(s[1])) : asf(s[0]) + asf(s[1]);
}
template <int CTRL> __device__ __forceinline__ float dpp(float v) {
  return asf(__builtin_amdgcn_mov_dpp(asu(v), CTRL, 0xf, 0xf, true));
}
constexpr int ROR4 = 0x124, ROR8 = 0x128, XOR1 = 0xB1, XOR2 = 0x4E, XOR7 = 0x141;
#define PIN() do { asm volatile("" ::: "memory"); __builtin_amdgcn_sched_barrier(0); } while (0)
template <int N> __device__ __forceinline__ void waitvm() { asm volatile("s_waitcnt vmcnt(%0)" ::"n"(N) : "memory"); }

__device__ __forceinline__ u32x4 colfrag(const u32x4* v, int j) {
  const u32 sel = (j & 1) ? 0x07060302u : 0x05040100u;
  const int d = j >> 1;
  u32x4 f;
  f[0] = __builtin_amdgcn_perm(v[1][d], v[0][d], sel);
  f[1] = __builtin_amdgcn_perm(v[3][d], v[2][d], sel);
  f[2] = __builtin_amdgcn_perm(v[5][d], v[4][d], sel);
  f[3] = __builtin_amdgcn_perm(v[7][d], v[6][d], sel);
  return f;
}
__device__ __forceinline__ float merge8(float m, float l, float& M, float& L) {
  M = m;
  M = fmaxf(M, dpp<XOR1>(M)); M = fmaxf(M, dpp<XOR2>(M)); M = fmaxf(M, dpp<XOR7>(M));
  const float sc = __builtin_amdgcn_exp2f(m - M);
  L = sc * l;
  L += dpp<XOR1>(L); L += dpp<XOR2>(L); L += dpp<XOR7>(L);
  return sc;
}

__global__ void __launch_bounds__(512, 2) gated_mix_decode_kernel(Params p) {
  __shared__ __attribute__((aligned(16))) unsigned char smem[L_END];
  LAS unsigned char* L = (LAS unsigned char*)smem;
  const int tid = threadIdx.x, lane = tid & 63, w = __builtin_amdgcn_readfirstlane(tid >> 6);
  const int g4 = lane >> 4, l16 = lane & 15;
  const int c = blockIdx.x;
  const int g = c & 31, s = c >> 5, br = g >> 4, b = (g >> 2) & 3, kvh = g & 3;
  const int kh = c & 1, cbk = (c >> 1) & 15, kvhq = (c >> 5) & 3, brq = c >> 7;
  const int sl0 = kh ? 12 + (w & 3) : w, sl1 = 8 + (w & 3);
  const bool has0 = !kh || w < 4, has1 = !kh && w < 4;
  const int m = b * 8 + s, hO = kvh * 8 + (m >> 2), ns = m & 3, rb = w >> 1;

  const rsrc_t rK = mkr(br ? p.k1 : p.k0, KV_BYTES), rV = mkr(br ? p.v1 : p.v0, KV_BYTES);
  const rsrc_t rWQ = mkr(brq ? p.wq1 : p.wq0, W_BYTES), rWO = mkr(br ? p.wo1 : p.wo0, W_BYTES);
  const rsrc_t rX = mkr(p.x, 4 * 1024 * 2), rG = mkr(p.gw, 1024 * 2), rWS = mkr(p.ws, WS_END);

  const int kvS = (b * 4 + kvh) * (8192 * 256) + (s * 256 + w * 4) * 1024;
  const int kvV = lane * 16;
  const int wqL = (32 * ((lane >> 3) & 1) + 8 * g4) * 8192 + (lane & 7) * 16;
  const int wqV0 = has0 ? wqL : OOB, wqV1 = has1 ? wqL : OOB;
  const int wqS0 = (64 * sl0) * 8192 + (kvhq * 1024 + 64 * cbk) * 2, wqS1 = (64 * sl1) * 8192 + (kvhq * 1024 + 64 * cbk) * 2;
  const int woV = (8 * g4) * 2048 + l16 * 16;
  const int woS = (hO * 128 + 32 * rb) * 2048 + (256 * ns + 128 * (w & 1)) * 2;
  u32x4 wq0[8], wq1[8], kd[32], vd[32], wo[8];
  auto issueK = [&](int j) __attribute__((always_inline)) { kd[j] = ldnt(rK, kvV, kvS + ((j >> 2) * 32 + (j & 3)) * 1024); PIN(); };
  auto issueV = [&](int u) __attribute__((always_inline)) { vd[u] = ldnt(rV, kvV, kvS + ((u >> 2) * 32 + (u & 3)) * 1024); PIN(); };

  const u32 epoch = __builtin_amdgcn_raw_buffer_load_b32(rWS, 0, WS_EPOCH, AUX_SC1);
  const int xo = 32 * ((l16 >> 2) & 1) + 8 * g4;
  const u32x4 xf0 = ldpl(rX, ((lane & 3) * 1024 + 64 * sl0 + xo) * 2);
  const u32x4 xf1 = ldpl(rX, ((lane & 3) * 1024 + 64 * sl1 + xo) * 2);
  const int gk = 128 * w + 32 * (l16 >> 2) + 8 * g4;
  const u32x4 xg = ldpl(rX, ((lane & 3) * 1024 + gk) * 2);
  const u32x4 gf = ldpl(rG, gk * 2);
  if (tid == 0) *(LAS u32*)(L + L_CNT) = 0u;
  PIN();
#pragma unroll
  for (int n = 0; n < 8; ++n) { wq0[n] = ldnt(rWQ, wqV0, wqS0 + n * 8192); PIN(); }
#pragma unroll
  for (int n = 0; n < 8; ++n) { wq1[n] = ldnt(rWQ, wqV1, wqS1 + n * 8192); PIN(); }
#pragma unroll
  for (int j = 0; j < K_EARLY; ++j) issueK(j);

  {
    f32x4 qa[8];
#pragma unroll
    for (int j = 0; j < 8; ++j) qa[j] = mfma(xf0, colfrag(wq0, j), f32x4{0.f, 0.f, 0.f, 0.f});
#pragma unroll
    for (int j = 0; j < 8; ++j) qa[j] = mfma(xf1, colfrag(wq1, j), qa[j]);
    if (g4 == ((lane >> 3) & 1) && g4 < 2) {
      LAS float* red = (LAS float*)(L + L_RED) + ((w * 2 + g4) * 4) * 64 + (lane & 7) * 8;
#pragma unroll
      for (int r = 0; r < 4; ++r) {
        *(LAS f32x4*)(red + r * 64) = f32x4{qa[0][r], qa[1][r], qa[2][r], qa[3][r]};
        *(LAS f32x4*)(red + r * 64 + 4) = f32x4{qa[4][r], qa[5][r], qa[6][r], qa[7][r]};
      }
    }
    float gs = 0.f;
#pragma unroll
    for (int d = 0; d < 4; ++d) gs += rbf(bflo(xg[d]) * bflo(gf[d])) + rbf(bfhi(xg[d]) * bfhi(gf[d]));
    gs += dpp<ROR4>(gs);
    gs += dpp<ROR8>(gs);
    gs = x32(x16(gs, false), false);
    if (lane < 4) ((LAS float*)(L + L_GP))[w * 4 + lane] = gs;
  }
  const u32 tag = epoch + 1u;
  __syncthreads();
  if (w < 4) {
    const LAS float* red = (const LAS float*)(L + L_RED) + w * 64 + lane;
    float a = 0.f;
#pragma unroll
    for (int i = 0; i < 16; ++i) a += red[i * 256];
    stc2(u32x2{asu(a), tag}, rWS, lane * 8, WS_Q + ((((brq * 4 + kvhq) * 4 + w) * 2 + kh) * 1024 + cbk * 64) * 8);
  }
  PIN();

  const int qS = WS_Q + (((br * 4 + kvh) * 4 + b) * 2048 + w * 128) * 8;
  u32x4 qg0, qg1;
#pragma unroll
  for (int j = K_EARLY; j < K_POLL; ++j) { waitvm<K_PACE - 1>(); issueK(j); }
  qg0 = ldc4(rWS, lane * 16, qS); qg1 = ldc4(rWS, lane * 16, qS + 8192);
  PIN();
#pragma unroll
  for (int j = K_POLL; j < 32; ++j) issueK(j);
  for (unsigned spin = 0; spin < kSpin; ++spin) {
    if (__all(qg0[1] == tag && qg0[3] == tag && qg1[1] == tag && qg1[3] == tag)) break;
    qg0 = ldc4(rWS, lane * 16, qS); qg1 = ldc4(rWS, lane * 16, qS + 8192);
    PIN();
  }
  ((LAS u32*)(L + L_QS))[tid] = pkbf(asf(qg0[0]) + asf(qg1[0]), asf(qg0[2]) + asf(qg1[2]));
  __syncthreads();
  u32x4 qf[4];
#pragma unroll
  for (int kk = 0; kk < 4; ++kk) qf[kk] = *(const LAS u32x4*)(L + L_QS + (l16 & 7) * 256 + kk * 64 + g4 * 16);

  constexpr float SCALE = 0.08838834764831845f, LOG2E = 1.4426950408889634f;
  f32x4 S[8];
  LAS unsigned char* kst = L + L_KST + w * 8192;
#pragma unroll
  for (int T = 0; T < 8; ++T) {
    LAS unsigned char* kb = kst + (T & 1) * 4096;
#pragma unroll
    for (int i = 0; i < 4; ++i) {
      const int rho = 4 * g4 + i;
      *(LAS u32x4*)(kb + rho * 256 + ((l16 ^ rho) * 16)) = kd[4 * T + i];
    }
    if (T < V_EARLY) {
      PIN();
#pragma unroll
      for (int i = 0; i < 4; ++i) issueV(4 * T + i);
    }
    __builtin_amdgcn_fence(__ATOMIC_RELEASE, "wavefront");
    __builtin_amdgcn_wave_barrier();
    __builtin_amdgcn_fence(__ATOMIC_ACQUIRE, "wavefront");
    f32x4 a = {0.f, 0.f, 0.f, 0.f};
#pragma unroll
    for (int kk = 0; kk < 4; ++kk) {
      const u32x4 kf = *(const LAS u32x4*)(kb + l16 * 256 + (((4 * kk + g4) ^ l16) * 16));
      a = mfma(kf, qf[kk], a);
    }
    const u32 r0 = pkbf(a[0], a[1]), r1 = pkbf(a[2], a[3]);
    const u32 t0 = pkbf(bflo(r0) * SCALE, bfhi(r0) * SCALE), t1 = pkbf(bflo(r1) * SCALE, bfhi(r1) * SCALE);
    S[T] = f32x4{bflo(t0), bfhi(t0), bflo(t1), bfhi(t1)};
  }
  float mx = S[0][0];
#pragma unroll
  for (int T = 0; T < 8; ++T)
#pragma unroll
    for (int r = 0; r < 4; ++r) mx = fmaxf(mx, S[T][r]);
  mx = x32(x16(mx, true), true);
  const float mxs = mx * LOG2E;
  float ls = 0.f;
  u32x4 pf[4];
#pragma unroll
  for (int T = 0; T < 8; ++T) {
    float e[4];
#pragma unroll
    for (int r = 0; r < 4; ++r) { e[r] = __builtin_amdgcn_exp2f(S[T][r] * LOG2E - mxs); ls += e[r]; }
    pf[T >> 1][(T & 1) * 2] = pkbf(e[0], e[1]);
    pf[T >> 1][(T & 1) * 2 + 1] = pkbf(e[2], e[3]);
  }
  ls = x32(x16(ls, false), false);
  if (lane < 8) *(LAS f32x2*)(L + L_ML + (w * 8 + lane) * 8) = f32x2{mxs, ls};
  PIN();
#pragma unroll
  for (int u = 4 * V_EARLY; u < 32; ++u) issueV(u);
  f32x4 acc[8];
#pragma unroll
  for (int j = 0; j < 8; ++j) acc[j] = f32x4{0.f, 0.f, 0.f, 0.f};
#pragma unroll
  for (int t = 0; t < 4; ++t) {
#pragma unroll
    for (int j = 0; j < 8; ++j) acc[j] = mfma(colfrag(vd + 8 * t, j), pf[t], acc[j]);
    PIN();
    if (t == 0) {
#pragma unroll
      for (int i = 0; i < 8; ++i) { wo[i] = ldnt(rWO, woV, woS + i * 2048); PIN(); }
    }
  }
  if (l16 < 8) {
    LAS float* pr = (LAS float*)(L + L_PART) + (w * 8 + l16) * PSTR + 32 * g4;
#pragma unroll
    for (int r = 0; r < 4; ++r) {
      *(LAS f32x4*)(pr + 8 * r) = f32x4{acc[0][r], acc[1][r], acc[2][r], acc[3][r]};
      *(LAS f32x4*)(pr + 8 * r + 4) = f32x4{acc[4][r], acc[5][r], acc[6][r], acc[7][r]};
    }
  }
  __syncthreads();
  if (lane < 32) {
    f32x4 o[8];
#pragma unroll
    for (int i = 0; i < 8; ++i) o[i] = *(const LAS f32x4*)((const LAS float*)(L + L_PART) + (i * 8 + w) * PSTR + 4 * lane);
    const f32x2 ml = *(const LAS f32x2*)(L + L_ML + ((lane & 7) * 8 + w) * 8);
    float M, Ls;
    const float sc = merge8(ml[0], ml[1], M, Ls);
    f32x4 t = {0.f, 0.f, 0.f, 0.f};
#pragma unroll
    for (int i = 0; i < 8; ++i) t += asf(__builtin_amdgcn_readlane(asu(sc), i)) * o[i];
    const int hh = br * 32 + kvh * 8 + w;
    stc4(u32x4{pkbf(t[0], t[1]), tag, pkbf(t[2], t[3]), tag}, rWS, WS_OD + (hh * 32 + ((b * 8 + s + 2 * w) & 31)) * 512 + lane * 16);
    if (lane == 0) stc4(u32x4{asu(M), tag, asu(Ls), tag}, rWS, WS_OS + (hh * 32 + b * 8 + s) * 16);
  }

  u32x4 wf[8];
#pragma unroll
  for (int j = 0; j < 8; ++j) { wf[j] = colfrag(wo, j); asm volatile("" : "+v"(wf[j])); }
  float gt, gn;
  {
    float z = 0.f;
#pragma unroll
    for (int i = 0; i < 8; ++i) z += ((const LAS float*)(L + L_GP))[i * 4 + (c >> 6)];
    z = rbf(z);
    gt = rbf(1.f / (1.f + __expf(-z)));
    gn = rbf(1.f - gt);
    asm volatile("" : "+v"(gt), "+v"(gn));
  }
  PIN();
  if (w < 4) {
    const int hh = br * 32 + hO;
    u32x4 d[4], sv;
    bool oks = false, okd[4] = {false, false, false, false};
    for (unsigned spin = 0; spin < kSpin; ++spin) {
      if (!oks) sv = ldc4(rWS, (lane & 7) * 16, WS_OS + (hh * 32 + w * 8) * 16);
#pragma unroll
      for (int j = 0; j < 4; ++j)
        if (!okd[j]) d[j] = ldc4(rWS, ((w * 8 + 2 * j + (lane >> 5) + 2 * (hO & 7)) & 31) * 512 + (lane & 31) * 16, WS_OD + hh * 32 * 512);
      oks = sv[1] == tag && sv[3] == tag;
      bool ok = oks;
#pragma unroll
      for (int j = 0; j < 4; ++j) { okd[j] = d[j][1] == tag && d[j][3] == tag; ok = ok && okd[j]; }
      PIN();
      const int missing = __builtin_popcountll(__ballot(!ok));
      if (missing == 0) break;
      if (missing > 32) __builtin_amdgcn_s_sleep(4);
    }
    float M, Ls;
    const float cf = merge8(asf(sv[0]), asf(sv[2]), M, Ls) * __builtin_amdgcn_rcpf(Ls);
    f32x4 o = {0.f, 0.f, 0.f, 0.f};
#pragma unroll
    for (int j = 0; j < 4; ++j) {
      const float c0 = asf(__builtin_amdgcn_readlane(asu(cf), 2 * j)), c1 = asf(__builtin_amdgcn_readlane(asu(cf), 2 * j + 1));
      const float cj = lane < 32 ? c0 : c1;
      o += cj * f32x4{bflo(d[j][0]), bfhi(d[j][0]), bflo(d[j][2]), bfhi(d[j][2])};
    }
#pragma unroll
    for (int e = 0; e < 4; ++e) o[e] = x32(o[e], false);
    if (lane < 32) *(LAS u32x2*)(L + L_OB + w * 256 + lane * 8) = u32x2{pkbf(o[0], o[1]), pkbf(o[2], o[3])};
  }
  __syncthreads();
  {
    const u32x4 of = *(const LAS u32x4*)(L + L_OB + (l16 & 3) * 256 + (32 * rb + 8 * g4) * 2);
    f32x4 oa[8];
#pragma unroll
    for (int j = 0; j < 8; ++j) oa[j] = mfma(of, wf[j], f32x4{0.f, 0.f, 0.f, 0.f});
    LAS f32x4* red = (LAS f32x4*)(L + L_RED) + w * 128 + l16;
    if (g4 == 0) { red[0] = oa[0]; red[16] = oa[1]; }
    else if (g4 == 1) { red[32] = oa[2]; red[48] = oa[3]; }
    else if (g4 == 2) { red[64] = oa[4]; red[80] = oa[5]; }
    else { red[96] = oa[6]; red[112] = oa[7]; }
  }
  __syncthreads();
  {
    const int bb = tid >> 7, cp = tid & 127, ch = cp >> 6, cc = (cp >> 2) & 15, j = (cp & 3) * 2;
    const LAS float* red = (const LAS float*)(L + L_RED) + (ch * 128 + j * 16 + cc) * 4 + bb;
    f32x2 o = {0.f, 0.f};
#pragma unroll
    for (int q = 0; q < 4; ++q) o += f32x2{red[q * 1024], red[q * 1024 + 64]};
    const int r = (bb * 4 + ns) * 16 + (cp >> 3);
    stc4(u32x4{asu(o[0]), tag, asu(o[1]), tag}, rWS, WS_F + ((r * 64 + br * 32 + ((hO + r) & 31)) * 16 + 2 * (cp & 7)) * 8);
  }

  {
    u32x4 d;
    bool ok = false;
    __builtin_amdgcn_s_sleep(16);
    for (unsigned spin = 0; spin < kSpin; ++spin) {
      if (!ok) {
        d = ldc4(rWS, lane * 16, WS_F + c * 8192 + w * 1024);
        ok = d[1] == tag && d[3] == tag;
      }
      PIN();
      const int missing = __builtin_popcountll(__ballot(!ok));
      if (missing == 0) break;
      if (missing > 16) __builtin_amdgcn_s_sleep(4);
    }
    float a0 = asf(d[0]), a1 = asf(d[2]);
    a0 = x32(x16(a0 + dpp<ROR8>(a0), false), false);
    a1 = x32(x16(a1 + dpp<ROR8>(a1), false), false);
    if (lane < 8) *(LAS f32x2*)(L + L_FIN + w * 64 + lane * 8) = f32x2{a0, a1};
    u32 old = 0;
    if (lane == 0) old = __hip_atomic_fetch_add((LAS u32*)(L + L_CNT), 1u, __ATOMIC_ACQ_REL, __HIP_MEMORY_SCOPE_WORKGROUP);
    if (__builtin_amdgcn_readfirstlane(old) == 7u) {
      const LAS f32x2* fin = (const LAS f32x2*)(L + L_FIN) + (lane & 7);
      const f32x2 a = (fin[0] + fin[8]) + (fin[16] + fin[24]);
      const f32x2 bq = (fin[32] + fin[40]) + (fin[48] + fin[56]);
      const float o0 = rbf(gt * rbf(a[0])) + rbf(gn * rbf(bq[0]));
      const float o1 = rbf(gt * rbf(a[1])) + rbf(gn * rbf(bq[1]));
      if (lane < 8)
        __builtin_amdgcn_raw_buffer_store_b32(pkbf(o0, o1), mkr(p.out, 8192), ((c >> 6) * 512 + ((c >> 4) & 3) * 128 + (c & 15) * 8 + lane) * 4, 0, AUX_SC1);
      if (c == 0 && lane == 0) __builtin_amdgcn_raw_buffer_store_b32(tag, rWS, 0, WS_EPOCH, AUX_SC1);
    }
  }
}

extern "C" void kernel_launch(void* const* d_in, const int* in_sizes, int n_in, void* d_out, int out_size, void* d_ws, size_t ws_size,
                              hipStream_t stream) {
  Params p;
  p.x = d_in[0]; p.gw = d_in[10];
  p.k0 = d_in[1]; p.v0 = d_in[2]; p.wq0 = d_in[3]; p.wo0 = d_in[4];
  p.k1 = d_in[6]; p.v1 = d_in[7]; p.wq1 = d_in[8]; p.wo1 = d_in[9];
  p.out = d_out; p.ws = d_ws;
  hipLaunchKernelGGL(gated_mix_decode_kernel, dim3(256), dim3(512), 0, stream, p);
}
```

```cpp
#include <hip/hip_runtime.h>
#include <hip/hip_bf16.h>
#include <stdint.h>

typedef unsigned int u32;
typedef u32 u32x2 __attribute__((ext_vector_type(2)));
typedef u32 u32x4 __attribute__((ext_vector_type(4)));
typedef float f32x2 __attribute__((ext_vector_type(2)));
typedef float f32x4 __attribute__((ext_vector_type(4)));
typedef short bf16x8 __attribute__((ext_vector_type(8)));
typedef __bf16 bf2_t __attribute__((ext_vector_type(2)));
typedef __amdgpu_buffer_rsrc_t rsrc_t;
#define LAS __attribute__((address_space(3)))

constexpr int KV_BYTES = 4 * 4 * 8192 * 128 * 2;
constexpr int W_BYTES = 1024 * 4096 * 2;
constexpr int AUX_NT = 2, AUX_SC1 = 16;
constexpr int K_EARLY = 4;
constexpr int K_PACE = 6;
constexpr int K_POLL = 16;
constexpr int V_EARLY = 6;
constexpr unsigned kSpin = 1u << 18;

constexpr int WS_EPOCH = 0;
constexpr int WS_Q = 4096;
constexpr int WS_OD = WS_Q + 2 * 4 * 4 * 2048 * 8;
constexpr int WS_OS = WS_OD + 2 * 32 * 4 * 8 * 64 * 8;
constexpr int WS_F = WS_OS + 2 * 32 * 4 * 8 * 16;
constexpr int WS_END = WS_F + 256 * 64 * 16 * 8;

constexpr int L_KST = 0;
constexpr int L_RED = L_KST + 8 * 8192;
constexpr int L_QS = L_RED + 16384;
constexpr int PSTR = 132;
constexpr int L_PART = L_QS + 2048;
constexpr int L_ML = L_PART + 8 * 8 * PSTR * 4;
constexpr int L_OB = L_ML + 512;
constexpr int L_FIN = L_OB + 1024;
constexpr int L_GP = L_FIN + 512;
constexpr int L_CNT = L_GP + 128;
constexpr int L_END = L_CNT + 16;

struct Params {
  const void* x; const void* gw;
  const void* k0; const void* v0; const void* wq0; const void* wo0;
  const void* k1; const void* v1; const void* wq1; const void* wo1;
  void* out; void* ws;
};

__device__ __forceinline__ rsrc_t mkr(const void* p, int bytes) { return __builtin_amdgcn_make_buffer_rsrc((void*)p, 0, bytes, 0x00020000); }
__device__ __forceinline__ u32x4 ldnt(rsrc_t r, int voff, int soff) { return __builtin_amdgcn_raw_buffer_load_b128(r, voff, soff, AUX_NT); }
__device__ __forceinline__ u32x4 ldpl(rsrc_t r, int voff) { return __builtin_amdgcn_raw_buffer_load_b128(r, voff, 0, 0); }
__device__ __forceinline__ u32x4 ldc4(rsrc_t r, int voff, int soff) { return __builtin_amdgcn_raw_buffer_load_b128(r, voff, soff, AUX_SC1); }
__device__ __forceinline__ void stc4(u32x4 v, rsrc_t r, int off) {
  __builtin_amdgcn_raw_buffer_store_b128(v, r, off, 0, AUX_SC1);
  __builtin_amdgcn_sched_barrier(0);
  asm volatile("s_nop 1");
  __builtin_amdgcn_sched_barrier(0);
}
__device__ __forceinline__ void stc2(u32x2 v, rsrc_t r, int voff, int soff) { __builtin_amdgcn_raw_buffer_store_b64(v, r, voff, soff, AUX_SC1); }
__device__ __forceinline__ f32x4 mfma(u32x4 a, u32x4 b, f32x4 c) {
  return __builtin_amdgcn_mfma_f32_16x16x32_bf16(__builtin_bit_cast(bf16x8, a), __builtin_bit_cast(bf16x8, b), c, 0, 0, 0);
}
__device__ __forceinline__ float asf(u32 u) { return __builtin_bit_cast(float, u); }
__device__ __forceinline__ u32 asu(float f) { return __builtin_bit_cast(u32, f); }
__device__ __forceinline__ u32 pkbf(float lo, float hi) {
  const f32x2 v = {lo, hi};
  return __builtin_bit_cast(u32, __builtin_convertvector(v, bf2_t));
}
__device__ __forceinline__ float bflo(u32 u) { return asf(u << 16); }
__device__ __forceinline__ float bfhi(u32 u) { return asf(u & 0xffff0000u); }
__device__ __forceinline__ float rbf(float f) { return bflo(pkbf(f, 0.f)); }
__device__ __forceinline__ float x16(float v, bool mx) {
  auto s = __builtin_amdgcn_permlane16_swap(asu(v), asu(v), false, false);
  return mx ? fmaxf(asf(s[0]), asf(s[1])) : asf(s[0]) + asf(s[1]);
}
__device__ __forceinline__ float x32(float v, bool mx) {
  auto s = __builtin_amdgcn_permlane32_swap(asu(v), asu(v), false, false);
  return mx ? fmaxf(asf(s[0]), asf(s[1])) : asf(s[0]) + asf(s[1]);
}
template <int CTRL> __device__ __forceinline__ float dpp(float v) {
  return asf(__builtin_amdgcn_mov_dpp(asu(v), CTRL, 0xf, 0xf, true));
}
constexpr int ROR4 = 0x124, ROR8 = 0x128, XOR1 = 0xB1, XOR2 = 0x4E, XOR7 = 0x141;
#define PIN() do { asm volatile("" ::: "memory"); __builtin_amdgcn_sched_barrier(0); } while (0)
template <int N> __device__ __forceinline__ void waitvm() { asm volatile("s_waitcnt vmcnt(%0)" ::"n"(N) : "memory"); }

__device__ __forceinline__ u32x4 colfrag(const u32x4* v, int j) {
  const u32 sel = (j & 1) ? 0x07060302u : 0x05040100u;
  const int d = j >> 1;
  u32x4 f;
  f[0] = __builtin_amdgcn_perm(v[1][d], v[0][d], sel);
  f[1] = __builtin_amdgcn_perm(v[3][d], v[2][d], sel);
  f[2] = __builtin_amdgcn_perm(v[5][d], v[4][d], sel);
  f[3] = __builtin_amdgcn_perm(v[7][d], v[6][d], sel);
  return f;
}
__device__ __forceinline__ float merge8(float m, float l, float& M, float& L) {
  M = m;
  M = fmaxf(M, dpp<XOR1>(M)); M = fmaxf(M, dpp<XOR2>(M)); M = fmaxf(M, dpp<XOR7>(M));
  const float sc = __builtin_amdgcn_exp2f(m - M);
  L = sc * l;
  L += dpp<XOR1>(L); L += dpp<XOR2>(L); L += dpp<XOR7>(L);
  return sc;
}

__global__ void __launch_bounds__(512, 2) gated_mix_decode_kernel(Params p) {
  __shared__ __attribute__((aligned(16))) unsigned char smem[L_END];
  LAS unsigned char* L = (LAS unsigned char*)smem;
  const int tid = threadIdx.x, lane = tid & 63, w = __builtin_amdgcn_readfirstlane(tid >> 6);
  const int g4 = lane >> 4, l16 = lane & 15;
  const int c = blockIdx.x;
  const int g = c & 31, s = c >> 5, br = g >> 4, b = (g >> 2) & 3, kvh = g & 3;
  const int m = b * 8 + s, hO = kvh * 8 + (m >> 2), ns = m & 3, rb = w >> 1;

  const rsrc_t rK = mkr(br ? p.k1 : p.k0, KV_BYTES), rV = mkr(br ? p.v1 : p.v0, KV_BYTES);
  const rsrc_t rWQ = mkr(br ? p.wq1 : p.wq0, W_BYTES), rWO = mkr(br ? p.wo1 : p.wo0, W_BYTES);
  const rsrc_t rX = mkr(p.x, 4 * 1024 * 2), rG = mkr(p.gw, 1024 * 2), rWS = mkr(p.ws, WS_END);

  const int kvS = (b * 4 + kvh) * (8192 * 256) + (s * 256 + w * 4) * 1024;
  const int kvV = lane * 16;
  const int wqV = (32 * ((lane >> 3) & 1) + 8 * g4) * 8192 + (lane & 7) * 16;
  const int wqS = (512 * (m & 1) + 64 * w) * 8192 + (kvh * 1024 + 64 * (m >> 1)) * 2;
  const int woV = (8 * g4) * 2048 + l16 * 16;
  const int woS = (hO * 128 + 32 * rb) * 2048 + (256 * ns + 128 * (w & 1)) * 2;
  u32x4 wq[8], kd[32], vd[32], wo[8];
  auto issueK = [&](int j) __attribute__((always_inline)) { kd[j] = ldnt(rK, kvV, kvS + ((j >> 2) * 32 + (j & 3)) * 1024); PIN(); };
  auto issueV = [&](int u) __attribute__((always_inline)) { vd[u] = ldnt(rV, kvV, kvS + ((u >> 2) * 32 + (u & 3)) * 1024); PIN(); };

  const u32 epoch = __builtin_amdgcn_raw_buffer_load_b32(rWS, 0, WS_EPOCH, AUX_SC1);
  const u32x4 xf = ldpl(rX, ((lane & 3) * 1024 + 512 * (m & 1) + 64 * w + 32 * ((l16 >> 2) & 1) + 8 * g4) * 2);
  const int gk = 128 * w + 32 * (l16 >> 2) + 8 * g4;
  const u32x4 xg = ldpl(rX, ((lane & 3) * 1024 + gk) * 2);
  const u32x4 gf = ldpl(rG, gk * 2);
  if (tid == 0) *(LAS u32*)(L + L_CNT) = 0u;
  PIN();
#pragma unroll
  for (int n = 0; n < 8; ++n) { wq[n] = ldnt(rWQ, wqV, wqS + n * 8192); PIN(); }
#pragma unroll
  for (int j = 0; j < K_EARLY; ++j) issueK(j);

  {
    f32x4 qa[8];
#pragma unroll
    for (int j = 0; j < 8; ++j) qa[j] = mfma(xf, colfrag(wq, j), f32x4{0.f, 0.f, 0.f, 0.f});
    if (g4 == ((lane >> 3) & 1) && g4 < 2) {
      LAS float* red = (LAS float*)(L + L_RED) + ((w * 2 + g4) * 4) * 64 + (lane & 7) * 8;
#pragma unroll
      for (int r = 0; r < 4; ++r) {
        *(LAS f32x4*)(red + r * 64) = f32x4{qa[0][r], qa[1][r], qa[2][r], qa[3][r]};
        *(LAS f32x4*)(red + r * 64 + 4) = f32x4{qa[4][r], qa[5][r], qa[6][r], qa[7][r]};
      }
    }
    float gs = 0.f;
#pragma unroll
    for (int d = 0; d < 4; ++d) gs += rbf(bflo(xg[d]) * bflo(gf[d])) + rbf(bfhi(xg[d]) * bfhi(gf[d]));
    gs += dpp<ROR4>(gs);
    gs += dpp<ROR8>(gs);
    gs = x32(x16(gs, false), false);
    if (lane < 4) ((LAS float*)(L + L_GP))[w * 4 + lane] = gs;
  }
  const u32 tag = epoch + 1u;
  __syncthreads();
  if (w < 4) {
    const LAS float* red = (const LAS float*)(L + L_RED) + w * 64 + lane;
    float a = 0.f;
#pragma unroll
    for (int i = 0; i < 16; ++i) a += red[i * 256];
    stc2(u32x2{asu(a), tag}, rWS, lane * 8, WS_Q + ((((br * 4 + kvh) * 4 + w) * 2 + (m & 1)) * 1024 + (m >> 1) * 64) * 8);
  }
  PIN();

  const int qS = WS_Q + (((br * 4 + kvh) * 4 + b) * 2048 + w * 128) * 8;
  u32x4 qg0, qg1;
#pragma unroll
  for (int j = K_EARLY; j < K_POLL; ++j) { waitvm<K_PACE - 1>(); issueK(j); }
  qg0 = ldc4(rWS, lane * 16, qS); qg1 = ldc4(rWS, lane * 16, qS + 8192);
  PIN();
#pragma unroll
  for (int j = K_POLL; j < 32; ++j) issueK(j);
  for (unsigned spin = 0; spin < kSpin; ++spin) {
    if (__all(qg0[1] == tag && qg0[3] == tag && qg1[1] == tag && qg1[3] == tag)) break;
    qg0 = ldc4(rWS, lane * 16, qS); qg1 = ldc4(rWS, lane * 16, qS + 8192);
    PIN();
  }
  ((LAS u32*)(L + L_QS))[tid] = pkbf(asf(qg0[0]) + asf(qg1[0]), asf(qg0[2]) + asf(qg1[2]));
  __syncthreads();
  u32x4 qf[4];
#pragma unroll
  for (int kk = 0; kk < 4; ++kk) qf[kk] = *(const LAS u32x4*)(L + L_QS + (l16 & 7) * 256 + kk * 64 + g4 * 16);

  constexpr float SCALE = 0.08838834764831845f, LOG2E = 1.4426950408889634f;
  f32x4 S[8];
  LAS unsigned char* kst = L + L_KST + w * 8192;
#pragma unroll
  for (int T = 0; T < 8; ++T) {
    LAS unsigned char* kb = kst + (T & 1) * 4096;
#pragma unroll
    for (int i = 0; i < 4; ++i) {
      const int rho = 4 * g4 + i;
      *(LAS u32x4*)(kb + rho * 256 + ((l16 ^ rho) * 16)) = kd[4 * T + i];
    }
    if (T < V_EARLY) {
      PIN();
#pragma unroll
      for (int i = 0; i < 4; ++i) issueV(4 * T + i);
    }
    __builtin_amdgcn_fence(__ATOMIC_RELEASE, "wavefront");
    __builtin_amdgcn_wave_barrier();
    __builtin_amdgcn_fence(__ATOMIC_ACQUIRE, "wavefront");
    f32x4 a = {0.f, 0.f, 0.f, 0.f};
#pragma unroll
    for (int kk = 0; kk < 4; ++kk) {
      const u32x4 kf = *(const LAS u32x4*)(kb + l16 * 256 + (((4 * kk + g4) ^ l16) * 16));
      a = mfma(kf, qf[kk], a);
    }
    const u32 r0 = pkbf(a[0], a[1]), r1 = pkbf(a[2], a[3]);
    const u32 t0 = pkbf(bflo(r0) * SCALE, bfhi(r0) * SCALE), t1 = pkbf(bflo(r1) * SCALE, bfhi(r1) * SCALE);
    S[T] = f32x4{bflo(t0), bfhi(t0), bflo(t1), bfhi(t1)};
  }
  float mx = S[0][0];
#pragma unroll
  for (int T = 0; T < 8; ++T)
#pragma unroll
    for (int r = 0; r < 4; ++r) mx = fmaxf(mx, S[T][r]);
  mx = x32(x16(mx, true), true);
  const float mxs = mx * LOG2E;
  float ls = 0.f;
  u32x4 pf[4];
#pragma unroll
  for (int T = 0; T < 8; ++T) {
    float e[4];
#pragma unroll
    for (int r = 0; r < 4; ++r) { e[r] = __builtin_amdgcn_exp2f(S[T][r] * LOG2E - mxs); ls += e[r]; }
    pf[T >> 1][(T & 1) * 2] = pkbf(e[0], e[1]);
    pf[T >> 1][(T & 1) * 2 + 1] = pkbf(e[2], e[3]);
  }
  ls = x32(x16(ls, false), false);
  if (lane < 8) *(LAS f32x2*)(L + L_ML + (w * 8 + lane) * 8) = f32x2{mxs, ls};
  PIN();
#pragma unroll
  for (int u = 4 * V_EARLY; u < 32; ++u) issueV(u);
  f32x4 acc[8];
#pragma unroll
  for (int j = 0; j < 8; ++j) acc[j] = f32x4{0.f, 0.f, 0.f, 0.f};
#pragma unroll
  for (int t = 0; t < 4; ++t) {
#pragma unroll
    for (int j = 0; j < 8; ++j) acc[j] = mfma(colfrag(vd + 8 * t, j), pf[t], acc[j]);
    PIN();
    if (t == 0) {
#pragma unroll
      for (int i = 0; i < 8; ++i) { wo[i] = ldnt(rWO, woV, woS + i * 2048); PIN(); }
    }
  }
  if (l16 < 8) {
    LAS float* pr = (LAS float*)(L + L_PART) + (w * 8 + l16) * PSTR + 32 * g4;
#pragma unroll
    for (int r = 0; r < 4; ++r) {
      *(LAS f32x4*)(pr + 8 * r) = f32x4{acc[0][r], acc[1][r], acc[2][r], acc[3][r]};
      *(LAS f32x4*)(pr + 8 * r + 4) = f32x4{acc[4][r], acc[5][r], acc[6][r], acc[7][r]};
    }
  }
  __syncthreads();
  if (lane < 32) {
    f32x4 o[8];
#pragma unroll
    for (int i = 0; i < 8; ++i) o[i] = *(const LAS f32x4*)((const LAS float*)(L + L_PART) + (i * 8 + w) * PSTR + 4 * lane);
    const f32x2 ml = *(const LAS f32x2*)(L + L_ML + ((lane & 7) * 8 + w) * 8);
    float M, Ls;
    const float sc = merge8(ml[0], ml[1], M, Ls);
    f32x4 t = {0.f, 0.f, 0.f, 0.f};
#pragma unroll
    for (int i = 0; i < 8; ++i) t += asf(__builtin_amdgcn_readlane(asu(sc), i)) * o[i];
    const int hh = br * 32 + kvh * 8 + w;
    stc4(u32x4{pkbf(t[0], t[1]), tag, pkbf(t[2], t[3]), tag}, rWS, WS_OD + (hh * 32 + ((b * 8 + s + 2 * w) & 31)) * 512 + lane * 16);
    if (lane == 0) stc4(u32x4{asu(M), tag, asu(Ls), tag}, rWS, WS_OS + (hh * 32 + b * 8 + s) * 16);
  }

  u32x4 wf[8];
#pragma unroll
  for (int j = 0; j < 8; ++j) { wf[j] = colfrag(wo, j); asm volatile("" : "+v"(wf[j])); }
  float gt, gn;
  {
    float z = 0.f;
#pragma unroll
    for (int i = 0; i < 8; ++i) z += ((const LAS float*)(L + L_GP))[i * 4 + (c >> 6)];
    z = rbf(z);
    gt = rbf(1.f / (1.f + __expf(-z)));
    gn = rbf(1.f - gt);
    asm volatile("" : "+v"(gt), "+v"(gn));
  }
  PIN();
  if (w < 4) {
    const int hh = br * 32 + hO;
    u32x4 d[4], sv;
    bool oks = false, okd[4] = {false, false, false, false};
    for (unsigned spin = 0; spin < kSpin; ++spin) {
      if (!oks) sv = ldc4(rWS, (lane & 7) * 16, WS_OS + (hh * 32 + w * 8) * 16);
#pragma unroll
      for (int j = 0; j < 4; ++j)
        if (!okd[j]) d[j] = ldc4(rWS, ((w * 8 + 2 * j + (lane >> 5) + 2 * (hO & 7)) & 31) * 512 + (lane & 31) * 16, WS_OD + hh * 32 * 512);
      oks = sv[1] == tag && sv[3] == tag;
      bool ok = oks;
#pragma unroll
      for (int j = 0; j < 4; ++j) { okd[j] = d[j][1] == tag && d[j][3] == tag; ok = ok && okd[j]; }
      PIN();
      const int missing = __builtin_popcountll(__ballot(!ok));
      if (missing == 0) break;
      if (missing > 32) __builtin_amdgcn_s_sleep(4);
    }
    float M, Ls;
    const float cf = merge8(asf(sv[0]), asf(sv[2]), M, Ls) * __builtin_amdgcn_rcpf(Ls);
    f32x4 o = {0.f, 0.f, 0.f, 0.f};
#pragma unroll
    for (int j = 0; j < 4; ++j) {
      const float c0 = asf(__builtin_amdgcn_readlane(asu(cf), 2 * j)), c1 = asf(__builtin_amdgcn_readlane(asu(cf), 2 * j + 1));
      const float cj = lane < 32 ? c0 : c1;
      o += cj * f32x4{bflo(d[j][0]), bfhi(d[j][0]), bflo(d[j][2]), bfhi(d[j][2])};
    }
#pragma unroll
    for (int e = 0; e < 4; ++e) o[e] = x32(o[e], false);
    if (lane < 32) *(LAS u32x2*)(L + L_OB + w * 256 + lane * 8) = u32x2{pkbf(o[0], o[1]), pkbf(o[2], o[3])};
  }
  __syncthreads();
  {
    const u32x4 of = *(const LAS u32x4*)(L + L_OB + (l16 & 3) * 256 + (32 * rb + 8 * g4) * 2);
    f32x4 oa[8];
#pragma unroll
    for (int j = 0; j < 8; ++j) oa[j] = mfma(of, wf[j], f32x4{0.f, 0.f, 0.f, 0.f});
    LAS f32x4* red = (LAS f32x4*)(L + L_RED) + w * 128 + l16;
    if (g4 == 0) { red[0] = oa[0]; red[16] = oa[1]; }
    else if (g4 == 1) { red[32] = oa[2]; red[48] = oa[3]; }
    else if (g4 == 2) { red[64] = oa[4]; red[80] = oa[5]; }
    else { red[96] = oa[6]; red[112] = oa[7]; }
  }
  __syncthreads();
  {
    const int bb = tid >> 7, cp = tid & 127, ch = cp >> 6, cc = (cp >> 2) & 15, j = (cp & 3) * 2;
    const LAS float* red = (const LAS float*)(L + L_RED) + (ch * 128 + j * 16 + cc) * 4 + bb;
    f32x2 o = {0.f, 0.f};
#pragma unroll
    for (int q = 0; q < 4; ++q) o += f32x2{red[q * 1024], red[q * 1024 + 64]};
    const int r = (bb * 4 + ns) * 16 + (cp >> 3);
    stc4(u32x4{asu(o[0]), tag, asu(o[1]), tag}, rWS, WS_F + ((r * 64 + br * 32 + ((hO + r) & 31)) * 16 + 2 * (cp & 7)) * 8);
  }

  {
    u32x4 d;
    bool ok = false;
    __builtin_amdgcn_s_sleep(16);
    for (unsigned spin = 0; spin < kSpin; ++spin) {
      if (!ok) {
        d = ldc4(rWS, lane * 16, WS_F + c * 8192 + w * 1024);
        ok = d[1] == tag && d[3] == tag;
      }
      PIN();
      const int missing = __builtin_popcountll(__ballot(!ok));
      if (missing == 0) break;
      if (missing > 16) __builtin_amdgcn_s_sleep(4);
    }
    float a0 = asf(d[0]), a1 = asf(d[2]);
    a0 = x32(x16(a0 + dpp<ROR8>(a0), false), false);
    a1 = x32(x16(a1 + dpp<ROR8>(a1), false), false);
    if (lane < 8) *(LAS f32x2*)(L + L_FIN + w * 64 + lane * 8) = f32x2{a0, a1};
    u32 old = 0;
    if (lane == 0) old = __hip_atomic_fetch_add((LAS u32*)(L + L_CNT), 1u, __ATOMIC_ACQ_REL, __HIP_MEMORY_SCOPE_WORKGROUP);
    if (__builtin_amdgcn_readfirstlane(old) == 7u) {
      const LAS f32x2* fin = (const LAS f32x2*)(L + L_FIN) + (lane & 7);
      const f32x2 a = (fin[0] + fin[8]) + (fin[16] + fin[24]);
      const f32x2 bq = (fin[32] + fin[40]) + (fin[48] + fin[56]);
      const float o0 = rbf(gt * rbf(a[0])) + rbf(gn * rbf(bq[0]));
      const float o1 = rbf(gt * rbf(a[1])) + rbf(gn * rbf(bq[1]));
      if (lane < 8)
        __builtin_amdgcn_raw_buffer_store_b32(pkbf(o0, o1), mkr(p.out, 8192), ((c >> 6) * 512 + ((c >> 4) & 3) * 128 + (c & 15) * 8 + lane) * 4, 0, AUX_SC1);
      if (c == 0 && lane == 0) __builtin_amdgcn_raw_buffer_store_b32(tag, rWS, 0, WS_EPOCH, AUX_SC1);
    }
  }
}

extern "C" void kernel_launch(void* const* d_in, const int* in_sizes, int n_in, void* d_out, int out_size, void* d_ws, size_t ws_size,
                              hipStream_t stream) {
  Params p;
  p.x = d_in[0]; p.gw = d_in[10];
  p.k0 = d_in[1]; p.v0 = d_in[2]; p.wq0 = d_in[3]; p.wo0 = d_in[4];
  p.k1 = d_in[6]; p.v1 = d_in[7]; p.wq1 = d_in[8]; p.wo1 = d_in[9];
  p.out = d_out; p.ws = d_ws;
  hipLaunchKernelGGL(gated_mix_decode_kernel, dim3(256), dim3(512), 0, stream, p);
}
```

```cpp
#include <hip/hip_runtime.h>
#include <hip/hip_bf16.h>
#include <stdint.h>

typedef unsigned int u32;
typedef u32 u32x2 __attribute__((ext_vector_type(2)));
typedef u32 u32x4 __attribute__((ext_vector_type(4)));
typedef float f32x2 __attribute__((ext_vector_type(2)));
typedef float f32x4 __attribute__((ext_vector_type(4)));
typedef short bf16x8 __attribute__((ext_vector_type(8)));
typedef __bf16 bf2_t __attribute__((ext_vector_type(2)));
typedef __amdgpu_buffer_rsrc_t rsrc_t;
#define LAS __attribute__((address_space(3)))

constexpr int KV_BYTES = 4 * 4 * 8192 * 128 * 2;
constexpr int W_BYTES = 1024 * 4096 * 2;
constexpr int AUX_NT = 2, AUX_SC1 = 16;
constexpr int OOB = 0x40000000;
constexpr int K_EARLY = 4;
constexpr int K_PACE = 6;
constexpr int K_POLL = 16;
constexpr int V_EARLY = 6;
constexpr unsigned kSpin = 1u << 18;

constexpr int WS_EPOCH = 0;
constexpr int WS_Q = 4096;
constexpr int WS_OD = WS_Q + 2 * 4 * 4 * 2048 * 8;
constexpr int WS_OS = WS_OD + 2 * 32 * 4 * 8 * 64 * 8;
constexpr int WS_F = WS_OS + 2 * 32 * 4 * 8 * 16;
constexpr int WS_END = WS_F + 256 * 64 * 16 * 8;

constexpr int L_KST = 0;
constexpr int L_RED = L_KST + 8 * 8192;
constexpr int L_QS = L_RED + 16384;
constexpr int PSTR = 132;
constexpr int L_PART = L_QS + 2048;
constexpr int L_ML = L_PART + 8 * 8 * PSTR * 4;
constexpr int L_OB = L_ML + 512;
constexpr int L_FIN = L_OB + 1024;
constexpr int L_GP = L_FIN + 512;
constexpr int L_CNT = L_GP + 128;
constexpr int L_END = L_CNT + 16;

struct Params {
  const void* x; const void* gw;
  const void* k0; const void* v0; const void* wq0; const void* wo0;
  const void* k1; const void* v1; const void* wq1; const void* wo1;
  void* out; void* ws;
};

__device__ __forceinline__ rsrc_t mkr(const void* p, int bytes) { return __builtin_amdgcn_make_buffer_rsrc((void*)p, 0, bytes, 0x00020000); }
__device__ __forceinline__ u32x4 ldnt(rsrc_t r, int voff, int soff) { return __builtin_amdgcn_raw_buffer_load_b128(r, voff, soff, AUX_NT); }
__device__ __forceinline__ u32x4 ldpl(rsrc_t r, int voff) { return __builtin_amdgcn_raw_buffer_load_b128(r, voff, 0, 0); }
__device__ __forceinline__ u32x4 ldc4(rsrc_t r, int voff, int soff) { return __builtin_amdgcn_raw_buffer_load_b128(r, voff, soff, AUX_SC1); }
__device__ __forceinline__ void stc4(u32x4 v, rsrc_t r, int off) {
  __builtin_amdgcn_raw_buffer_store_b128(v, r, off, 0, AUX_SC1);
  __builtin_amdgcn_sched_barrier(0);
  asm volatile("s_nop 1");
  __builtin_amdgcn_sched_barrier(0);
}
__device__ __forceinline__ void stc2(u32x2 v, rsrc_t r, int voff, int soff) { __builtin_amdgcn_raw_buffer_store_b64(v, r, voff, soff, AUX_SC1); }
__device__ __forceinline__ f32x4 mfma(u32x4 a, u32x4 b, f32x4 c) {
  return __builtin_amdgcn_mfma_f32_16x16x32_bf16(__builtin_bit_cast(bf16x8, a), __builtin_bit_cast(bf16x8, b), c, 0, 0, 0);
}
__device__ __forceinline__ float asf(u32 u) { return __builtin_bit_cast(float, u); }
__device__ __forceinline__ u32 asu(float f) { return __builtin_bit_cast(u32, f); }
__device__ __forceinline__ u32 pkbf(float lo, float hi) {
  const f32x2 v = {lo, hi};
  return __builtin_bit_cast(u32, __builtin_convertvector(v, bf2_t));
}
__device__ __forceinline__ float bflo(u32 u) { return asf(u << 16); }
__device__ __forceinline__ float bfhi(u32 u) { return asf(u & 0xffff0000u); }
__device__ __forceinline__ float rbf(float f) { return bflo(pkbf(f, 0.f)); }
__device__ __forceinline__ float x16(float v, bool mx) {
  auto s = __builtin_amdgcn_permlane16_swap(asu(v), asu(v), false, false);
  return mx ? fmaxf(asf(s[0]), asf(s[1])) : asf(s[0]) + asf(s[1]);
}
__device__ __forceinline__ float x32(float v, bool mx) {
  auto s = __builtin_amdgcn_permlane32_swap(asu(v), asu(v), false, false);
  return mx ? fmaxf(asf(s[0]), asf(s[1])) : asf(s[0]) + asf(s[1]);
}
template <int CTRL> __device__ __forceinline__ float dpp(float v) {
  return asf(__builtin_amdgcn_mov_dpp(asu(v), CTRL, 0xf, 0xf, true));
}
constexpr int ROR4 = 0x124, ROR8 = 0x128, XOR1 = 0xB1, XOR2 = 0x4E, XOR7 = 0x141;
#define PIN() do { asm volatile("" ::: "memory"); __builtin_amdgcn_sched_barrier(0); } while (0)
template <int N> __device__ __forceinline__ void waitvm() { asm volatile("s_waitcnt vmcnt(%0)" ::"n"(N) : "memory"); }

__device__ __forceinline__ u32x4 colfrag(const u32x4* v, int j) {
  const u32 sel = (j & 1) ? 0x07060302u : 0x05040100u;
  const int d = j >> 1;
  u32x4 f;
  f[0] = __builtin_amdgcn_perm(v[1][d], v[0][d], sel);
  f[1] = __builtin_amdgcn_perm(v[3][d], v[2][d], sel);
  f[2] = __builtin_amdgcn_perm(v[5][d], v[4][d], sel);
  f[3] = __builtin_amdgcn_perm(v[7][d], v[6][d], sel);
  return f;
}
__device__ __forceinline__ float merge8(float m, float l, float& M, float& L) {
  M = m;
  M = fmaxf(M, dpp<XOR1>(M)); M = fmaxf(M, dpp<XOR2>(M)); M = fmaxf(M, dpp<XOR7>(M));
  const float sc = __builtin_amdgcn_exp2f(m - M);
  L = sc * l;
  L += dpp<XOR1>(L); L += dpp<XOR2>(L); L += dpp<XOR7>(L);
  return sc;
}

__global__ void __launch_bounds__(512, 2) gated_mix_decode_kernel(Params p) {
  __shared__ __attribute__((aligned(16))) unsigned char smem[L_END];
  LAS unsigned char* L = (LAS unsigned char*)smem;
  const int tid = threadIdx.x, lane = tid & 63, w = __builtin_amdgcn_readfirstlane(tid >> 6);
  const int g4 = lane >> 4, l16 = lane & 15;
  const int c = blockIdx.x;
  const int g = c & 31, s = c >> 5, br = g >> 4, b = (g >> 2) & 3, kvh = g & 3;
  const int m = b * 8 + s, hO = kvh * 8 + (m >> 2), ns = m & 3, rb = w >> 1;

  const rsrc_t rK = mkr(br ? p.k1 : p.k0, KV_BYTES), rV = mkr(br ? p.v1 : p.v0, KV_BYTES);
  const rsrc_t rWQ = mkr(br ? p.wq1 : p.wq0, W_BYTES), rWO = mkr(br ? p.wo1 : p.wo0, W_BYTES);
  const rsrc_t rX = mkr(p.x, 4 * 1024 * 2), rG = mkr(p.gw, 1024 * 2), rWS = mkr(p.ws, WS_END);

  const int kvS = (b * 4 + kvh) * (8192 * 256) + (s * 256 + w * 4) * 1024;
  const int kvV = lane * 16;
  const int wqV = (32 * ((lane >> 3) & 1) + 8 * g4) * 8192 + (lane & 7) * 16;
  const int wqS = (512 * (m & 1) + 64 * w) * 8192 + (kvh * 1024 + 64 * (m >> 1)) * 2;
  const int woV = (8 * g4) * 2048 + l16 * 16;
  const int woS = (hO * 128 + 32 * rb) * 2048 + (256 * ns + 128 * (w & 1)) * 2;
  u32x4 wq[8], kd[32], vd[32], wo[8];
  auto issueK = [&](int j) __attribute__((always_inline)) { kd[j] = ldnt(rK, kvV, kvS + ((j >> 2) * 32 + (j & 3)) * 1024); PIN(); };
  auto issueV = [&](int u) __attribute__((always_inline)) { vd[u] = ldnt(rV, kvV, kvS + ((u >> 2) * 32 + (u & 3)) * 1024); PIN(); };

#pragma unroll
  for (int n = 0; n < 8; ++n) { wq[n] = ldnt(rWQ, wqV, wqS + n * 8192); PIN(); }
  const u32x4 xf = ldpl(rX, ((lane & 3) * 1024 + 512 * (m & 1) + 64 * w + 32 * ((l16 >> 2) & 1) + 8 * g4) * 2);
  const u32 epoch = __builtin_amdgcn_raw_buffer_load_b32(rWS, 0, WS_EPOCH, AUX_SC1);
  PIN();
#pragma unroll
  for (int j = 0; j < K_EARLY; ++j) issueK(j);
  const int gk = 128 * w + 32 * (l16 >> 2) + 8 * g4;
  const u32x4 xg = ldpl(rX, ((lane & 3) * 1024 + gk) * 2);
  const u32x4 gf = ldpl(rG, gk * 2);
  if (tid == 0) *(LAS u32*)(L + L_CNT) = 0u;
  PIN();

  {
    f32x4 qa[8];
#pragma unroll
    for (int j = 0; j < 8; ++j) qa[j] = mfma(xf, colfrag(wq, j), f32x4{0.f, 0.f, 0.f, 0.f});
    if (g4 == ((lane >> 3) & 1) && g4 < 2) {
      LAS float* red = (LAS float*)(L + L_RED) + ((w * 2 + g4) * 4) * 64 + (lane & 7) * 8;
#pragma unroll
      for (int r = 0; r < 4; ++r) {
        *(LAS f32x4*)(red + r * 64) = f32x4{qa[0][r], qa[1][r], qa[2][r], qa[3][r]};
        *(LAS f32x4*)(red + r * 64 + 4) = f32x4{qa[4][r], qa[5][r], qa[6][r], qa[7][r]};
      }
    }
  }
  const u32 tag = epoch + 1u;
  __syncthreads();
  if (w < 4) {
    const LAS float* red = (const LAS float*)(L + L_RED) + w * 64 + lane;
    float a = 0.f;
#pragma unroll
    for (int i = 0; i < 16; ++i) a += red[i * 256];
    stc2(u32x2{asu(a), tag}, rWS, lane * 8, WS_Q + ((((br * 4 + kvh) * 4 + w) * 2 + (m & 1)) * 1024 + (m >> 1) * 64) * 8);
  }
  PIN();
  {
    float gs = 0.f;
#pragma unroll
    for (int d = 0; d < 4; ++d) gs += rbf(bflo(xg[d]) * bflo(gf[d])) + rbf(bfhi(xg[d]) * bfhi(gf[d]));
    gs += dpp<ROR4>(gs);
    gs += dpp<ROR8>(gs);
    gs = x32(x16(gs, false), false);
    if (lane < 4) ((LAS float*)(L + L_GP))[w * 4 + lane] = gs;
  }
  PIN();
  const rsrc_t rO = mkr(p.out, 8192);
  const u32 tc0 = __builtin_amdgcn_raw_buffer_load_b32(rV, w == 1 ? 0 : OOB, kvS, AUX_NT);
  const u32 tc1 = __builtin_amdgcn_raw_buffer_load_b32(rWO, w == 2 ? 0 : OOB, woS, AUX_NT);
  const u32 tc2 = __builtin_amdgcn_raw_buffer_load_b32(rWS, w == 3 ? lane * 65536 : OOB, 0, AUX_SC1);
  const u32 tc3 = __builtin_amdgcn_raw_buffer_load_b32(rO, w == 4 ? 0 : OOB, 0, AUX_SC1);
  PIN();

  const int qS = WS_Q + (((br * 4 + kvh) * 4 + b) * 2048 + w * 128) * 8;
  u32x4 qg0, qg1;
#pragma unroll
  for (int j = K_EARLY; j < K_POLL; ++j) { waitvm<K_PACE - 1>(); issueK(j); }
  qg0 = ldc4(rWS, lane * 16, qS); qg1 = ldc4(rWS, lane * 16, qS + 8192);
  PIN();
#pragma unroll
  for (int j = K_POLL; j < 32; ++j) issueK(j);
  for (unsigned spin = 0; spin < kSpin; ++spin) {
    if (__all(qg0[1] == tag && qg0[3] == tag && qg1[1] == tag && qg1[3] == tag)) break;
    qg0 = ldc4(rWS, lane * 16, qS); qg1 = ldc4(rWS, lane * 16, qS + 8192);
    PIN();
  }
  ((LAS u32*)(L + L_QS))[tid] = pkbf(asf(qg0[0]) + asf(qg1[0]), asf(qg0[2]) + asf(qg1[2]));
  asm volatile("" ::"v"(tc0), "v"(tc1), "v"(tc2), "v"(tc3));
  __syncthreads();
  u32x4 qf[4];
#pragma unroll
  for (int kk = 0; kk < 4; ++kk) qf[kk] = *(const LAS u32x4*)(L + L_QS + (l16 & 7) * 256 + kk * 64 + g4 * 16);

  constexpr float SCALE = 0.08838834764831845f, LOG2E = 1.4426950408889634f;
  f32x4 S[8];
  LAS unsigned char* kst = L + L_KST + w * 8192;
#pragma unroll
  for (int T = 0; T < 8; ++T) {
    LAS unsigned char* kb = kst + (T & 1) * 4096;
#pragma unroll
    for (int i = 0; i < 4; ++i) {
      const int rho = 4 * g4 + i;
      *(LAS u32x4*)(kb + rho * 256 + ((l16 ^ rho) * 16)) = kd[4 * T + i];
    }
    if (T < V_EARLY) {
      PIN();
#pragma unroll
      for (int i = 0; i < 4; ++i) issueV(4 * T + i);
    }
    __builtin_amdgcn_fence(__ATOMIC_RELEASE, "wavefront");
    __builtin_amdgcn_wave_barrier();
    __builtin_amdgcn_fence(__ATOMIC_ACQUIRE, "wavefront");
    f32x4 a = {0.f, 0.f, 0.f, 0.f};
#pragma unroll
    for (int kk = 0; kk < 4; ++kk) {
      const u32x4 kf = *(const LAS u32x4*)(kb + l16 * 256 + (((4 * kk + g4) ^ l16) * 16));
      a = mfma(kf, qf[kk], a);
    }
    const u32 r0 = pkbf(a[0], a[1]), r1 = pkbf(a[2], a[3]);
    const u32 t0 = pkbf(bflo(r0) * SCALE, bfhi(r0) * SCALE), t1 = pkbf(bflo(r1) * SCALE, bfhi(r1) * SCALE);
    S[T] = f32x4{bflo(t0), bfhi(t0), bflo(t1), bfhi(t1)};
  }
  float mx = S[0][0];
#pragma unroll
  for (int T = 0; T < 8; ++T)
#pragma unroll
    for (int r = 0; r < 4; ++r) mx = fmaxf(mx, S[T][r]);
  mx = x32(x16(mx, true), true);
  const float mxs = mx * LOG2E;
  float ls = 0.f;
  u32x4 pf[4];
#pragma unroll
  for (int T = 0; T < 8; ++T) {
    float e[4];
#pragma unroll
    for (int r = 0; r < 4; ++r) { e[r] = __builtin_amdgcn_exp2f(S[T][r] * LOG2E - mxs); ls += e[r]; }
    pf[T >> 1][(T & 1) * 2] = pkbf(e[0], e[1]);
    pf[T >> 1][(T & 1) * 2 + 1] = pkbf(e[2], e[3]);
  }
  ls = x32(x16(ls, false), false);
  if (lane < 8) *(LAS f32x2*)(L + L_ML + (w * 8 + lane) * 8) = f32x2{mxs, ls};
  PIN();
#pragma unroll
  for (int u = 4 * V_EARLY; u < 32; ++u) issueV(u);
  f32x4 acc[8];
#pragma unroll
  for (int j = 0; j < 8; ++j) acc[j] = f32x4{0.f, 0.f, 0.f, 0.f};
#pragma unroll
  for (int t = 0; t < 4; ++t) {
#pragma unroll
    for (int j = 0; j < 8; ++j) acc[j] = mfma(colfrag(vd + 8 * t, j), pf[t], acc[j]);
    PIN();
    if (t == 0) {
#pragma unroll
      for (int i = 0; i < 8; ++i) { wo[i] = ldnt(rWO, woV, woS + i * 2048); PIN(); }
    }
  }
  if (l16 < 8) {
    LAS float* pr = (LAS float*)(L + L_PART) + (w * 8 + l16) * PSTR + 32 * g4;
#pragma unroll
    for (int r = 0; r < 4; ++r) {
      *(LAS f32x4*)(pr + 8 * r) = f32x4{acc[0][r], acc[1][r], acc[2][r], acc[3][r]};
      *(LAS f32x4*)(pr + 8 * r + 4) = f32x4{acc[4][r], acc[5][r], acc[6][r], acc[7][r]};
    }
  }
  __syncthreads();
  if (lane < 32) {
    f32x4 o[8];
#pragma unroll
    for (int i = 0; i < 8; ++i) o[i] = *(const LAS f32x4*)((const LAS float*)(L + L_PART) + (i * 8 + w) * PSTR + 4 * lane);
    const f32x2 ml = *(const LAS f32x2*)(L + L_ML + ((lane & 7) * 8 + w) * 8);
    float M, Ls;
    const float sc = merge8(ml[0], ml[1], M, Ls);
    f32x4 t = {0.f, 0.f, 0.f, 0.f};
#pragma unroll
    for (int i = 0; i < 8; ++i) t += asf(__builtin_amdgcn_readlane(asu(sc), i)) * o[i];
    const int hh = br * 32 + kvh * 8 + w;
    stc4(u32x4{pkbf(t[0], t[1]), tag, pkbf(t[2], t[3]), tag}, rWS, WS_OD + (hh * 32 + ((b * 8 + s + 2 * w) & 31)) * 512 + lane * 16);
    if (lane == 0) stc4(u32x4{asu(M), tag, asu(Ls), tag}, rWS, WS_OS + (hh * 32 + b * 8 + s) * 16);
  }

  u32x4 wf[8];
#pragma unroll
  for (int j = 0; j < 8; ++j) { wf[j] = colfrag(wo, j); asm volatile("" : "+v"(wf[j])); }
  float gt, gn;
  {
    float z = 0.f;
#pragma unroll
    for (int i = 0; i < 8; ++i) z += ((const LAS float*)(L + L_GP))[i * 4 + (c >> 6)];
    z = rbf(z);
    gt = rbf(1.f / (1.f + __expf(-z)));
    gn = rbf(1.f - gt);
    asm volatile("" : "+v"(gt), "+v"(gn));
  }
  PIN();
  if (w < 4) {
    const int hh = br * 32 + hO;
    u32x4 d[4], sv;
    bool oks = false, okd[4] = {false, false, false, false};
    for (unsigned spin = 0; spin < kSpin; ++spin) {
      if (!oks) sv = ldc4(rWS, (lane & 7) * 16, WS_OS + (hh * 32 + w * 8) * 16);
#pragma unroll
      for (int j = 0; j < 4; ++j)
        if (!okd[j]) d[j] = ldc4(rWS, ((w * 8 + 2 * j + (lane >> 5) + 2 * (hO & 7)) & 31) * 512 + (lane & 31) * 16, WS_OD + hh * 32 * 512);
      oks = sv[1] == tag && sv[3] == tag;
      bool ok = oks;
#pragma unroll
      for (int j = 0; j < 4; ++j) { okd[j] = d[j][1] == tag && d[j][3] == tag; ok = ok && okd[j]; }
      PIN();
      const int missing = __builtin_popcountll(__ballot(!ok));
      if (missing == 0) break;
      if (missing > 32) __builtin_amdgcn_s_sleep(4);
    }
    float M, Ls;
    const float cf = merge8(asf(sv[0]), asf(sv[2]), M, Ls) * __builtin_amdgcn_rcpf(Ls);
    f32x4 o = {0.f, 0.f, 0.f, 0.f};
#pragma unroll
    for (int j = 0; j < 4; ++j) {
      const float c0 = asf(__builtin_amdgcn_readlane(asu(cf), 2 * j)), c1 = asf(__builtin_amdgcn_readlane(asu(cf), 2 * j + 1));
      const float cj = lane < 32 ? c0 : c1;
      o += cj * f32x4{bflo(d[j][0]), bfhi(d[j][0]), bflo(d[j][2]), bfhi(d[j][2])};
    }
#pragma unroll
    for (int e = 0; e < 4; ++e) o[e] = x32(o[e], false);
    if (lane < 32) *(LAS u32x2*)(L + L_OB + w * 256 + lane * 8) = u32x2{pkbf(o[0], o[1]), pkbf(o[2], o[3])};
  }
  __syncthreads();
  {
    const u32x4 of = *(const LAS u32x4*)(L + L_OB + (l16 & 3) * 256 + (32 * rb + 8 * g4) * 2);
    f32x4 oa[8];
#pragma unroll
    for (int j = 0; j < 8; ++j) oa[j] = mfma(of, wf[j], f32x4{0.f, 0.f, 0.f, 0.f});
    LAS f32x4* red = (LAS f32x4*)(L + L_RED) + w * 128 + l16;
    if (g4 == 0) { red[0] = oa[0]; red[16] = oa[1]; }
    else if (g4 == 1) { red[32] = oa[2]; red[48] = oa[3]; }
    else if (g4 == 2) { red[64] = oa[4]; red[80] = oa[5]; }
    else { red[96] = oa[6]; red[112] = oa[7]; }
  }
  __syncthreads();
  {
    const int bb = tid >> 7, cp = tid & 127, ch = cp >> 6, cc = (cp >> 2) & 15, j = (cp & 3) * 2;
    const LAS float* red = (const LAS float*)(L + L_RED) + (ch * 128 + j * 16 + cc) * 4 + bb;
    f32x2 o = {0.f, 0.f};
#pragma unroll
    for (int q = 0; q < 4; ++q) o += f32x2{red[q * 1024], red[q * 1024 + 64]};
    const int r = (bb * 4 + ns) * 16 + (cp >> 3);
    stc4(u32x4{asu(o[0]), tag, asu(o[1]), tag}, rWS, WS_F + ((r * 64 + br * 32 + ((hO + r) & 31)) * 16 + 2 * (cp & 7)) * 8);
  }

  {
    u32x4 d;
    bool ok = false;
    __builtin_amdgcn_s_sleep(16);
    for (unsigned spin = 0; spin < kSpin; ++spin) {
      if (!ok) {
        d = ldc4(rWS, lane * 16, WS_F + c * 8192 + w * 1024);
        ok = d[1] == tag && d[3] == tag;
      }
      PIN();
      const int missing = __builtin_popcountll(__ballot(!ok));
      if (missing == 0) break;
      if (missing > 16) __builtin_amdgcn_s_sleep(4);
    }
    float a0 = asf(d[0]), a1 = asf(d[2]);
    a0 = x32(x16(a0 + dpp<ROR8>(a0), false), false);
    a1 = x32(x16(a1 + dpp<ROR8>(a1), false), false);
    if (lane < 8) *(LAS f32x2*)(L + L_FIN + w * 64 + lane * 8) = f32x2{a0, a1};
    u32 old = 0;
    if (lane == 0) old = __hip_atomic_fetch_add((LAS u32*)(L + L_CNT), 1u, __ATOMIC_ACQ_REL, __HIP_MEMORY_SCOPE_WORKGROUP);
    if (__builtin_amdgcn_readfirstlane(old) == 7u) {
      const LAS f32x2* fin = (const LAS f32x2*)(L + L_FIN) + (lane & 7);
      const f32x2 a = (fin[0] + fin[8]) + (fin[16] + fin[24]);
      const f32x2 bq = (fin[32] + fin[40]) + (fin[48] + fin[56]);
      const float o0 = rbf(gt * rbf(a[0])) + rbf(gn * rbf(bq[0]));
      const float o1 = rbf(gt * rbf(a[1])) + rbf(gn * rbf(bq[1]));
      if (lane < 8)
        __builtin_amdgcn_raw_buffer_store_b32(pkbf(o0, o1), rO, ((c >> 6) * 512 + ((c >> 4) & 3) * 128 + (c & 15) * 8 + lane) * 4, 0, AUX_SC1);
      if (c == 0 && lane == 0) __builtin_amdgcn_raw_buffer_store_b32(tag, rWS, 0, WS_EPOCH, AUX_SC1);
    }
  }
}

extern "C" void kernel_launch(void* const* d_in, const int* in_sizes, int n_in, void* d_out, int out_size, void* d_ws, size_t ws_size,
                              hipStream_t stream) {
  Params p;
  p.x = d_in[0]; p.gw = d_in[10];
  p.k0 = d_in[1]; p.v0 = d_in[2]; p.wq0 = d_in[3]; p.wo0 = d_in[4];
  p.k1 = d_in[6]; p.v1 = d_in[7]; p.wq1 = d_in[8]; p.wo1 = d_in[9];
  p.out = d_out; p.ws = d_ws;
  hipLaunchKernelGGL(gated_mix_decode_kernel, dim3(256), dim3(512), 0, stream, p);
}
```

```cpp
#include <hip/hip_runtime.h>
#include <hip/hip_bf16.h>
#include <stdint.h>

typedef unsigned int u32;
typedef u32 u32x2 __attribute__((ext_vector_type(2)));
typedef u32 u32x4 __attribute__((ext_vector_type(4)));
typedef float f32x2 __attribute__((ext_vector_type(2)));
typedef float f32x4 __attribute__((ext_vector_type(4)));
typedef short bf16x8 __attribute__((ext_vector_type(8)));
typedef __bf16 bf2_t __attribute__((ext_vector_type(2)));
typedef __amdgpu_buffer_rsrc_t rsrc_t;
#define LAS __attribute__((address_space(3)))

constexpr int KV_BYTES = 4 * 4 * 8192 * 128 * 2;
constexpr int W_BYTES = 1024 * 4096 * 2;
constexpr int AUX_NT = 2, AUX_SC1 = 16;
constexpr int K_EARLY = 4;
constexpr int K_PACE = 6;
constexpr int K_POLL = 16;
constexpr int V_EARLY = 6;
constexpr unsigned kSpin = 1u << 18;

constexpr int WS_EPOCH = 0;
constexpr int WS_Q = 4096;
constexpr int WS_OD = WS_Q + 2 * 4 * 4 * 2048 * 8;
constexpr int WS_OS = WS_OD + 2 * 32 * 4 * 8 * 64 * 8;
constexpr int WS_F = WS_OS + 2 * 32 * 4 * 8 * 16;
constexpr int WS_END = WS_F + 256 * 64 * 16 * 8;

constexpr int L_KST = 0;
constexpr int L_RED = L_KST + 8 * 8192;
constexpr int L_QS = L_RED + 16384;
constexpr int PSTR = 132;
constexpr int L_PART = L_QS + 2048;
constexpr int L_ML = L_PART + 8 * 8 * PSTR * 4;
constexpr int L_OB = L_ML + 512;
constexpr int L_FIN = L_OB + 1024;
constexpr int L_GP = L_FIN + 512;
constexpr int L_CNT = L_GP + 128;
constexpr int L_END = L_CNT + 16;

struct Params {
  const void* x; const void* gw;
  const void* k0; const void* v0; const void* wq0; const void* wo0;
  const void* k1; const void* v1; const void* wq1; const void* wo1;
  void* out; void* ws;
};

__device__ __forceinline__ rsrc_t mkr(const void* p, int bytes) { return __builtin_amdgcn_make_buffer_rsrc((void*)p, 0, bytes, 0x00020000); }
__device__ __forceinline__ u32x4 ldnt(rsrc_t r, int voff, int soff) { return __builtin_amdgcn_raw_buffer_load_b128(r, voff, soff, AUX_NT); }
__device__ __forceinline__ u32x4 ldpl(rsrc_t r, int voff) { return __builtin_amdgcn_raw_buffer_load_b128(r, voff, 0, 0); }
__device__ __forceinline__ u32x4 ldc4(rsrc_t r, int voff, int soff) { return __builtin_amdgcn_raw_buffer_load_b128(r, voff, soff, AUX_SC1); }
__device__ __forceinline__ void stc4(u32x4 v, rsrc_t r, int off) {
  __builtin_amdgcn_raw_buffer_store_b128(v, r, off, 0, AUX_SC1);
  __builtin_amdgcn_sched_barrier(0);
  asm volatile("s_nop 1");
  __builtin_amdgcn_sched_barrier(0);
}
__device__ __forceinline__ void stc2(u32x2 v, rsrc_t r, int voff, int soff) { __builtin_amdgcn_raw_buffer_store_b64(v, r, voff, soff, AUX_SC1); }
__device__ __forceinline__ f32x4 mfma(u32x4 a, u32x4 b, f32x4 c) {
  return __builtin_amdgcn_mfma_f32_16x16x32_bf16(__builtin_bit_cast(bf16x8, a), __builtin_bit_cast(bf16x8, b), c, 0, 0, 0);
}
__device__ __forceinline__ float asf(u32 u) { return __builtin_bit_cast(float, u); }
__device__ __forceinline__ u32 asu(float f) { return __builtin_bit_cast(u32, f); }
__device__ __forceinline__ u32 pkbf(float lo, float hi) {
  const f32x2 v = {lo, hi};
  return __builtin_bit_cast(u32, __builtin_convertvector(v, bf2_t));
}
__device__ __forceinline__ float bflo(u32 u) { return asf(u << 16); }
__device__ __forceinline__ float bfhi(u32 u) { return asf(u & 0xffff0000u); }
__device__ __forceinline__ float rbf(float f) { return bflo(pkbf(f, 0.f)); }
__device__ __forceinline__ float x16(float v, bool mx) {
  auto s = __builtin_amdgcn_permlane16_swap(asu(v), asu(v), false, false);
  return mx ? fmaxf(asf(s[0]), asf(s[1])) : asf(s[0]) + asf(s[1]);
}
__device__ __forceinline__ float x32(float v, bool mx) {
  auto s = __builtin_amdgcn_permlane32_swap(asu(v), asu(v), false, false);
  return mx ? fmaxf(asf(s[0]), asf(s[1])) : asf(s[0]) + asf(s[1]);
}
template <int CTRL> __device__ __forceinline__ float dpp(float v) {
  return asf(__builtin_amdgcn_mov_dpp(asu(v), CTRL, 0xf, 0xf, true));
}
constexpr int ROR4 = 0x124, ROR8 = 0x128, XOR1 = 0xB1, XOR2 = 0x4E, XOR7 = 0x141;
#define PIN() do { asm volatile("" ::: "memory"); __builtin_amdgcn_sched_barrier(0); } while (0)
template <int N> __device__ __forceinline__ void waitvm() { asm volatile("s_waitcnt vmcnt(%0)" ::"n"(N) : "memory"); }

__device__ __forceinline__ u32x4 colfrag(const u32x4* v, int j) {
  const u32 sel = (j & 1) ? 0x07060302u : 0x05040100u;
  const int d = j >> 1;
  u32x4 f;
  f[0] = __builtin_amdgcn_perm(v[1][d], v[0][d], sel);
  f[1] = __builtin_amdgcn_perm(v[3][d], v[2][d], sel);
  f[2] = __builtin_amdgcn_perm(v[5][d], v[4][d], sel);
  f[3] = __builtin_amdgcn_perm(v[7][d], v[6][d], sel);
  return f;
}
__device__ __forceinline__ float merge8(float m, float l, float& M, float& L) {
  M = m;
  M = fmaxf(M, dpp<XOR1>(M)); M = fmaxf(M, dpp<XOR2>(M)); M = fmaxf(M, dpp<XOR7>(M));
  const float sc = __builtin_amdgcn_exp2f(m - M);
  L = sc * l;
  L += dpp<XOR1>(L); L += dpp<XOR2>(L); L += dpp<XOR7>(L);
  return sc;
}

__global__ void __launch_bounds__(512, 2) gated_mix_decode_kernel(Params p) {
  __shared__ __attribute__((aligned(16))) unsigned char smem[L_END];
  LAS unsigned char* L = (LAS unsigned char*)smem;
  const int tid = threadIdx.x, lane = tid & 63, w = __builtin_amdgcn_readfirstlane(tid >> 6);
  const int g4 = lane >> 4, l16 = lane & 15;
  const int c = blockIdx.x;
  const int g = c & 31, s = c >> 5, br = g >> 4, b = (g >> 2) & 3, kvh = g & 3;
  const int m = b * 8 + s, hO = kvh * 8 + (m >> 2), ns = m & 3, rb = w >> 1;

  const rsrc_t rK = mkr(br ? p.k1 : p.k0, KV_BYTES), rV = mkr(br ? p.v1 : p.v0, KV_BYTES);
  const rsrc_t rWQ = mkr(br ? p.wq1 : p.wq0, W_BYTES), rWO = mkr(br ? p.wo1 : p.wo0, W_BYTES);
  const rsrc_t rX = mkr(p.x, 4 * 1024 * 2), rG = mkr(p.gw, 1024 * 2), rWS = mkr(p.ws, WS_END);

  const int kvS = (b * 4 + kvh) * (8192 * 256) + (s * 256 + w * 4) * 1024;
  const int kvV = lane * 16;
  const int wqV = (32 * ((lane >> 3) & 1) + 8 * g4) * 8192 + (lane & 7) * 16;
  const int wqS = (512 * (m & 1) + 64 * w) * 8192 + (kvh * 1024 + 64 * (m >> 1)) * 2;
  const int woV = (8 * g4) * 2048 + l16 * 16;
  const int woS = (hO * 128 + 32 * rb) * 2048 + (256 * ns + 128 * (w & 1)) * 2;
  u32x4 wq[8], kd[32], vd[32], wo[8];
  auto issueK = [&](int j) __attribute__((always_inline)) { kd[j] = ldnt(rK, kvV, kvS + ((j >> 2) * 32 + (j & 3)) * 1024); PIN(); };
  auto issueV = [&](int u) __attribute__((always_inline)) { vd[u] = ldnt(rV, kvV, kvS + ((u >> 2) * 32 + (u & 3)) * 1024); PIN(); };

#pragma unroll
  for (int n = 0; n < 8; ++n) { wq[n] = ldnt(rWQ, wqV, wqS + n * 8192); PIN(); }
  const u32x4 xf = ldpl(rX, ((lane & 3) * 1024 + 512 * (m & 1) + 64 * w + 32 * ((l16 >> 2) & 1) + 8 * g4) * 2);
  const u32 epoch = __builtin_amdgcn_raw_buffer_load_b32(rWS, 0, WS_EPOCH, AUX_SC1);
  PIN();
#pragma unroll
  for (int j = 0; j < K_EARLY; ++j) issueK(j);
  const int gk = 128 * w + 32 * (l16 >> 2) + 8 * g4;
  const u32x4 xg = ldpl(rX, ((lane & 3) * 1024 + gk) * 2);
  const u32x4 gf = ldpl(rG, gk * 2);
  if (tid == 0) *(LAS u32*)(L + L_CNT) = 0u;
  PIN();

  {
    f32x4 qa[8];
#pragma unroll
    for (int j = 0; j < 8; ++j) qa[j] = mfma(xf, colfrag(wq, j), f32x4{0.f, 0.f, 0.f, 0.f});
    if (g4 == ((lane >> 3) & 1) && g4 < 2) {
      LAS float* red = (LAS float*)(L + L_RED) + ((w * 2 + g4) * 4) * 64 + (lane & 7) * 8;
#pragma unroll
      for (int r = 0; r < 4; ++r) {
        *(LAS f32x4*)(red + r * 64) = f32x4{qa[0][r], qa[1][r], qa[2][r], qa[3][r]};
        *(LAS f32x4*)(red + r * 64 + 4) = f32x4{qa[4][r], qa[5][r], qa[6][r], qa[7][r]};
      }
    }
  }
  const u32 tag = epoch + 1u;
  __syncthreads();
  if (w < 4) {
    const LAS float* red = (const LAS float*)(L + L_RED) + w * 64 + lane;
    float a = 0.f;
#pragma unroll
    for (int i = 0; i < 16; ++i) a += red[i * 256];
    stc2(u32x2{asu(a), tag}, rWS, lane * 8, WS_Q + ((((br * 4 + kvh) * 4 + w) * 2 + (m & 1)) * 1024 + (m >> 1) * 64) * 8);
  }
  PIN();
  {
    float gs = 0.f;
#pragma unroll
    for (int d = 0; d < 4; ++d) gs += rbf(bflo(xg[d]) * bflo(gf[d])) + rbf(bfhi(xg[d]) * bfhi(gf[d]));
    gs += dpp<ROR4>(gs);
    gs += dpp<ROR8>(gs);
    gs = x32(x16(gs, false), false);
    if (lane < 4) ((LAS float*)(L + L_GP))[w * 4 + lane] = gs;
  }
  PIN();

  const int qS = WS_Q + (((br * 4 + kvh) * 4 + b) * 2048 + w * 128) * 8;
  u32x4 qg0, qg1;
#pragma unroll
  for (int j = K_EARLY; j < K_POLL; ++j) { waitvm<K_PACE - 1>(); issueK(j); }
  qg0 = ldc4(rWS, lane * 16, qS); qg1 = ldc4(rWS, lane * 16, qS + 8192);
  PIN();
#pragma unroll
  for (int j = K_POLL; j < 32; ++j) issueK(j);
  for (unsigned spin = 0; spin < kSpin; ++spin) {
    if (__all(qg0[1] == tag && qg0[3] == tag && qg1[1] == tag && qg1[3] == tag)) break;
    qg0 = ldc4(rWS, lane * 16, qS); qg1 = ldc4(rWS, lane * 16, qS + 8192);
    PIN();
  }
  ((LAS u32*)(L + L_QS))[tid] = pkbf(asf(qg0[0]) + asf(qg1[0]), asf(qg0[2]) + asf(qg1[2]));
  __syncthreads();
  u32x4 qf[4];
#pragma unroll
  for (int kk = 0; kk < 4; ++kk) qf[kk] = *(const LAS u32x4*)(L + L_QS + (l16 & 7) * 256 + kk * 64 + g4 * 16);

  constexpr float SCALE = 0.08838834764831845f, LOG2E = 1.4426950408889634f;
  f32x4 S[8];
  LAS unsigned char* kst = L + L_KST + w * 8192;
#pragma unroll
  for (int T = 0; T < 8; ++T) {
    LAS unsigned char* kb = kst + (T & 1) * 4096;
#pragma unroll
    for (int i = 0; i < 4; ++i) {
      const int rho = 4 * g4 + i;
      *(LAS u32x4*)(kb + rho * 256 + ((l16 ^ rho) * 16)) = kd[4 * T + i];
    }
    if (T < V_EARLY) {
      PIN();
#pragma unroll
      for (int i = 0; i < 4; ++i) issueV(4 * T + i);
    }
    __builtin_amdgcn_fence(__ATOMIC_RELEASE, "wavefront");
    __builtin_amdgcn_wave_barrier();
    __builtin_amdgcn_fence(__ATOMIC_ACQUIRE, "wavefront");
    f32x4 a = {0.f, 0.f, 0.f, 0.f};
#pragma unroll
    for (int kk = 0; kk < 4; ++kk) {
      const u32x4 kf = *(const LAS u32x4*)(kb + l16 * 256 + (((4 * kk + g4) ^ l16) * 16));
      a = mfma(kf, qf[kk], a);
    }
    const u32 r0 = pkbf(a[0], a[1]), r1 = pkbf(a[2], a[3]);
    const u32 t0 = pkbf(bflo(r0) * SCALE, bfhi(r0) * SCALE), t1 = pkbf(bflo(r1) * SCALE, bfhi(r1) * SCALE);
    S[T] = f32x4{bflo(t0), bfhi(t0), bflo(t1), bfhi(t1)};
  }
  float mx = S[0][0];
#pragma unroll
  for (int T = 0; T < 8; ++T)
#pragma unroll
    for (int r = 0; r < 4; ++r) mx = fmaxf(mx, S[T][r]);
  mx = x32(x16(mx, true), true);
  const float mxs = mx * LOG2E;
  float ls = 0.f;
  u32x4 pf[4];
#pragma unroll
  for (int T = 0; T < 8; ++T) {
    float e[4];
#pragma unroll
    for (int r = 0; r < 4; ++r) { e[r] = __builtin_amdgcn_exp2f(S[T][r] * LOG2E - mxs); ls += e[r]; }
    pf[T >> 1][(T & 1) * 2] = pkbf(e[0], e[1]);
    pf[T >> 1][(T & 1) * 2 + 1] = pkbf(e[2], e[3]);
  }
  ls = x32(x16(ls, false), false);
  if (lane < 8) *(LAS f32x2*)(L + L_ML + (w * 8 + lane) * 8) = f32x2{mxs, ls};
  PIN();
#pragma unroll
  for (int u = 4 * V_EARLY; u < 32; ++u) issueV(u);
  f32x4 acc[8];
#pragma unroll
  for (int j = 0; j < 8; ++j) acc[j] = f32x4{0.f, 0.f, 0.f, 0.f};
#pragma unroll
  for (int t = 0; t < 4; ++t) {
#pragma unroll
    for (int j = 0; j < 8; ++j) acc[j] = mfma(colfrag(vd + 8 * t, j), pf[t], acc[j]);
    PIN();
    if (t == 0) {
#pragma unroll
      for (int i = 0; i < 8; ++i) { wo[i] = ldnt(rWO, woV, woS + i * 2048); PIN(); }
    }
  }
  if (l16 < 8) {
    LAS float* pr = (LAS float*)(L + L_PART) + (w * 8 + l16) * PSTR + 32 * g4;
#pragma unroll
    for (int r = 0; r < 4; ++r) {
      *(LAS f32x4*)(pr + 8 * r) = f32x4{acc[0][r], acc[1][r], acc[2][r], acc[3][r]};
      *(LAS f32x4*)(pr + 8 * r + 4) = f32x4{acc[4][r], acc[5][r], acc[6][r], acc[7][r]};
    }
  }
  __syncthreads();
  if (lane < 32) {
    f32x4 o[8];
#pragma unroll
    for (int i = 0; i < 8; ++i) o[i] = *(const LAS f32x4*)((const LAS float*)(L + L_PART) + (i * 8 + w) * PSTR + 4 * lane);
    const f32x2 ml = *(const LAS f32x2*)(L + L_ML + ((lane & 7) * 8 + w) * 8);
    float M, Ls;
    const float sc = merge8(ml[0], ml[1], M, Ls);
    f32x4 t = {0.f, 0.f, 0.f, 0.f};
#pragma unroll
    for (int i = 0; i < 8; ++i) t += asf(__builtin_amdgcn_readlane(asu(sc), i)) * o[i];
    const int hh = br * 32 + kvh * 8 + w;
    stc4(u32x4{pkbf(t[0], t[1]), tag, pkbf(t[2], t[3]), tag}, rWS, WS_OD + (hh * 32 + ((b * 8 + s + 2 * w) & 31)) * 512 + lane * 16);
    if (lane == 0) stc4(u32x4{asu(M), tag, asu(Ls), tag}, rWS, WS_OS + (hh * 32 + b * 8 + s) * 16);
  }

  u32x4 wf[8];
#pragma unroll
  for (int j = 0; j < 8; ++j) { wf[j] = colfrag(wo, j); asm volatile("" : "+v"(wf[j])); }
  float gt, gn;
  {
    float z = 0.f;
#pragma unroll
    for (int i = 0; i < 8; ++i) z += ((const LAS float*)(L + L_GP))[i * 4 + (c >> 6)];
    z = rbf(z);
    gt = rbf(1.f / (1.f + __expf(-z)));
    gn = rbf(1.f - gt);
    asm volatile("" : "+v"(gt), "+v"(gn));
  }
  PIN();
  if (w < 4) {
    const int hh = br * 32 + hO;
    u32x4 d[4], sv;
    bool oks = false, okd[4] = {false, false, false, false};
    for (unsigned spin = 0; spin < kSpin; ++spin) {
      if (!oks) sv = ldc4(rWS, (lane & 7) * 16, WS_OS + (hh * 32 + w * 8) * 16);
#pragma unroll
      for (int j = 0; j < 4; ++j)
        if (!okd[j]) d[j] = ldc4(rWS, ((w * 8 + 2 * j + (lane >> 5) + 2 * (hO & 7)) & 31) * 512 + (lane & 31) * 16, WS_OD + hh * 32 * 512);
      oks = sv[1] == tag && sv[3] == tag;
      bool ok = oks;
#pragma unroll
      for (int j = 0; j < 4; ++j) { okd[j] = d[j][1] == tag && d[j][3] == tag; ok = ok && okd[j]; }
      PIN();
      const int missing = __builtin_popcountll(__ballot(!ok));
      if (missing == 0) break;
      if (missing > 32) __builtin_amdgcn_s_sleep(4);
    }
    float M, Ls;
    const float cf = merge8(asf(sv[0]), asf(sv[2]), M, Ls) * __builtin_amdgcn_rcpf(Ls);
    f32x4 o = {0.f, 0.f, 0.f, 0.f};
#pragma unroll
    for (int j = 0; j < 4; ++j) {
      const float c0 = asf(__builtin_amdgcn_readlane(asu(cf), 2 * j)), c1 = asf(__builtin_amdgcn_readlane(asu(cf), 2 * j + 1));
      const float cj = lane < 32 ? c0 : c1;
      o += cj * f32x4{bflo(d[j][0]), bfhi(d[j][0]), bflo(d[j][2]), bfhi(d[j][2])};
    }
#pragma unroll
    for (int e = 0; e < 4; ++e) o[e] = x32(o[e], false);
    if (lane < 32) *(LAS u32x2*)(L + L_OB + w * 256 + lane * 8) = u32x2{pkbf(o[0], o[1]), pkbf(o[2], o[3])};
  }
  __syncthreads();
  {
    const u32x4 of = *(const LAS u32x4*)(L + L_OB + (l16 & 3) * 256 + (32 * rb + 8 * g4) * 2);
    f32x4 oa[8];
#pragma unroll
    for (int j = 0; j < 8; ++j) oa[j] = mfma(of, wf[j], f32x4{0.f, 0.f, 0.f, 0.f});
    LAS f32x4* red = (LAS f32x4*)(L + L_RED) + w * 128 + l16;
    if (g4 == 0) { red[0] = oa[0]; red[16] = oa[1]; }
    else if (g4 == 1) { red[32] = oa[2]; red[48] = oa[3]; }
    else if (g4 == 2) { red[64] = oa[4]; red[80] = oa[5]; }
    else { red[96] = oa[6]; red[112] = oa[7]; }
  }
  __syncthreads();
  {
    const int bb = tid >> 7, cp = tid & 127, ch = cp >> 6, cc = (cp >> 2) & 15, j = (cp & 3) * 2;
    const LAS float* red = (const LAS float*)(L + L_RED) + (ch * 128 + j * 16 + cc) * 4 + bb;
    f32x2 o = {0.f, 0.f};
#pragma unroll
    for (int q = 0; q < 4; ++q) o += f32x2{red[q * 1024], red[q * 1024 + 64]};
    const int r = (bb * 4 + ns) * 16 + (cp >> 3);
    stc4(u32x4{asu(o[0]), tag, asu(o[1]), tag}, rWS, WS_F + ((r * 64 + br * 32 + ((hO + r) & 31)) * 16 + 2 * (cp & 7)) * 8);
  }

  {
    u32x4 d;
    bool ok = false;
    __builtin_amdgcn_s_sleep(16);
    for (unsigned spin = 0; spin < kSpin; ++spin) {
      if (!ok) {
        d = ldc4(rWS, lane * 16, WS_F + c * 8192 + w * 1024);
        ok = d[1] == tag && d[3] == tag;
      }
      PIN();
      const int missing = __builtin_popcountll(__ballot(!ok));
      if (missing == 0) break;
      if (missing > 16) __builtin_amdgcn_s_sleep(4);
    }
    float a0 = asf(d[0]), a1 = asf(d[2]);
    a0 = x32(x16(a0 + dpp<ROR8>(a0), false), false);
    a1 = x32(x16(a1 + dpp<ROR8>(a1), false), false);
    if (lane < 8) *(LAS f32x2*)(L + L_FIN + w * 64 + lane * 8) = f32x2{a0, a1};
    u32 old = 0;
    if (lane == 0) old = __hip_atomic_fetch_add((LAS u32*)(L + L_CNT), 1u, __ATOMIC_ACQ_REL, __HIP_MEMORY_SCOPE_WORKGROUP);
    if (__builtin_amdgcn_readfirstlane(old) == 7u) {
      const LAS f32x2* fin = (const LAS f32x2*)(L + L_FIN) + (lane & 7);
      const f32x2 a = (fin[0] + fin[8]) + (fin[16] + fin[24]);
      const f32x2 bq = (fin[32] + fin[40]) + (fin[48] + fin[56]);
      const float o0 = rbf(gt * rbf(a[0])) + rbf(gn * rbf(bq[0]));
      const float o1 = rbf(gt * rbf(a[1])) + rbf(gn * rbf(bq[1]));
      if (lane < 8)
        __builtin_amdgcn_raw_buffer_store_b32(pkbf(o0, o1), mkr(p.out, 8192), ((c >> 6) * 512 + ((c >> 4) & 3) * 128 + (c & 15) * 8 + lane) * 4, 0, AUX_SC1);
      if (c == 0 && lane == 0) __builtin_amdgcn_raw_buffer_store_b32(tag, rWS, 0, WS_EPOCH, AUX_SC1);
    }
  }
}

extern "C" void kernel_launch(void* const* d_in, const int* in_sizes, int n_in, void* d_out, int out_size, void* d_ws, size_t ws_size,
                              hipStream_t stream) {
  Params p;
  p.x = d_in[0]; p.gw = d_in[10];
  p.k0 = d_in[1]; p.v0 = d_in[2]; p.wq0 = d_in[3]; p.wo0 = d_in[4];
  p.k1 = d_in[6]; p.v1 = d_in[7]; p.wq1 = d_in[8]; p.wo1 = d_in[9];
  p.out = d_out; p.ws = d_ws;
  hipLaunchKernelGGL(gated_mix_decode_kernel, dim3(256), dim3(512), 0, stream, p);
}
```
